```python
import torch

def _softmax(x, axis=-1):
    return torch.softmax(x, dim=axis)
def _gelu(x):
    return torch.nn.functional.gelu(x, approximate="tanh")
def _silu(x):
    return torch.nn.functional.silu(x)
def _sigmoid(x):
    return torch.sigmoid(x)
def _layernorm(x, axis=-1, eps=1e-5):
    m = x.mean(dim=axis, keepdim=True)
    v = x.var(dim=axis, keepdim=True, unbiased=False)
    return (x - m) / torch.sqrt(v + eps)
def _scatter_add(tgt, idx, src, axis=0):
    return tgt.clone().index_add_(axis, idx, src)
def _segment_sum(src, seg, n):
    out = torch.zeros((n,) + src.shape[1:], dtype=src.dtype)
    return out.index_add_(0, seg, src)
def _pad(x, pw):
    flat = tuple(v for pair in reversed(pw) for v in pair)
    return torch.nn.functional.pad(x, flat)

kernel_name = 'concat_heads__mha_block_cached__mha_block_cached__near_ridge'

def setup_inputs():

    _w = (lambda a: (lambda t: t.to(torch.bfloat16) if t.is_floating_point() else t)(torch.as_tensor(a)))
    def _A_setup():
        B, Sq, D = 16, 32, 1024
        H, Sk, Dh = 32, 32, 128
        return {
            "x": _w((lambda *s: torch.randn(*s, dtype=torch.bfloat16) * 0.8)(B, Sq, D)),
            "k_cache": _w((lambda *s: torch.randn(*s, dtype=torch.bfloat16) * 0.8)(B, H, Sk, Dh)),
            "v_cache": _w((lambda *s: torch.randn(*s, dtype=torch.bfloat16) * 0.05)(B, H, Sk, Dh)),
            "wq": _w((lambda *s: torch.randn(*s, dtype=torch.bfloat16) * 0.8)(D, H * Dh) * (D ** -0.5)),
            "wo": _w((lambda *s: torch.randn(*s, dtype=torch.bfloat16) * 0.05)(H * Dh, D) * ((H * Dh) ** -0.5)),
        }
    _A_in = _A_setup()

    def _B_setup():
        B, Sq, D = 16, 32, 1024
        H, Sk, Dh = 32, 32, 128
        return {
            "x": _w((lambda *s: torch.randn(*s, dtype=torch.bfloat16) * 0.8)(B, Sq, D)),
            "k_cache": _w((lambda *s: torch.randn(*s, dtype=torch.bfloat16) * 0.8)(B, H, Sk, Dh)),
            "v_cache": _w((lambda *s: torch.randn(*s, dtype=torch.bfloat16) * 0.05)(B, H, Sk, Dh)),
            "wq": _w((lambda *s: torch.randn(*s, dtype=torch.bfloat16) * 0.8)(D, H * Dh) * (D ** -0.5)),
            "wo": _w((lambda *s: torch.randn(*s, dtype=torch.bfloat16) * 0.05)(H * Dh, D) * ((H * Dh) ** -0.5)),
        }
    _B_in = _B_setup()

    return {
        "A_x": _A_in["x"],
        "A_k_cache": _A_in["k_cache"],
        "A_v_cache": _A_in["v_cache"],
        "A_wq": _A_in["wq"],
        "A_wo": _A_in["wo"],
        "B_x": _B_in["x"],
        "B_k_cache": _B_in["k_cache"],
        "B_v_cache": _B_in["v_cache"],
        "B_wq": _B_in["wq"],
        "B_wo": _B_in["wo"],
    }

def reference(A_x, A_k_cache, A_v_cache, A_wq, A_wo, B_x, B_k_cache, B_v_cache, B_wq, B_wo):
    def _A_ref(x, k_cache, v_cache, wq, wo):
        B, Sq, D = x.shape
        H, Dh = 32, 128
        q = (lambda x, *d: x.permute(*d))((x @ wq).reshape(B, Sq, H, Dh), 0, 2, 1, 3)
        scores = torch.einsum("bhqd,bhkd->bhqk", q, k_cache) * (Dh ** -0.5)
        p = _softmax(scores, axis=-1)
        o = torch.einsum("bhqk,bhkd->bhqd", p, v_cache)
        return (lambda x, *d: x.permute(*d))(o, 0, 2, 1, 3).reshape(B, Sq, H * Dh) @ wo

    def _B_ref(x, k_cache, v_cache, wq, wo):
        B, Sq, D = x.shape
        H, Dh = 32, 128
        q = (lambda x, *d: x.permute(*d))((x @ wq).reshape(B, Sq, H, Dh), 0, 2, 1, 3)
        scores = torch.einsum("bhqd,bhkd->bhqk", q, k_cache) * (Dh ** -0.5)
        p = _softmax(scores, axis=-1)
        o = torch.einsum("bhqk,bhkd->bhqd", p, v_cache)
        return (lambda x, *d: x.permute(*d))(o, 0, 2, 1, 3).reshape(B, Sq, H * Dh) @ wo

    a = _A_ref(A_x, A_k_cache, A_v_cache, A_wq, A_wo)
    b = _B_ref(B_x, B_k_cache, B_v_cache, B_wq, B_wo)
    return torch.concatenate([a, b], axis=-1)
```

```cpp
#include <hip/hip_runtime.h>
#include <hip/hip_bf16.h>
typedef unsigned short u16;
__device__ __forceinline__ float bf2f(u16 x){ return __uint_as_float(((unsigned)x)<<16); }
__device__ __forceinline__ u16 f2bf(float x){ __hip_bfloat16 b = __float2bfloat16(x); return *(u16*)&b; }

__global__ void __launch_bounds__(128) k1(const u16* x0,const u16* k0,const u16* v0,const u16* wq0,
                   const u16* x1,const u16* k1_,const u16* v1,const u16* wq1, u16* O){
  int blk=blockIdx.x; int br=blk>>9; int b=(blk>>5)&15; int h=blk&31;
  const u16* x=br?x1:x0; const u16* kc=br?k1_:k0; const u16* vc=br?v1:v0; const u16* wq=br?wq1:wq0;
  __shared__ float q[32][129]; __shared__ float p[32][33];
  int d=threadIdx.x;
  float acc[32];
  for(int i=0;i<32;i++)acc[i]=0.f;
  for(int k=0;k<1024;k++){
    float w=bf2f(wq[(size_t)k*4096+h*128+d]);
    #pragma unroll
    for(int i=0;i<32;i++) acc[i]+=bf2f(x[(size_t)(b*32+i)*1024+k])*w;
  }
  #pragma unroll
  for(int i=0;i<32;i++) q[i][d]=bf2f(f2bf(acc[i]));
  __syncthreads();
  const u16* kk=kc+(size_t)(b*32+h)*4096; const u16* vv=vc+(size_t)(b*32+h)*4096;
  for(int e=d;e<1024;e+=128){ int i=e>>5,j=e&31; float s=0; for(int t=0;t<128;t++) s+=q[i][t]*bf2f(kk[j*128+t]);
    s=bf2f(f2bf(s)); s=bf2f(f2bf(s*0.08838834764831845f)); p[i][j]=s; }
  __syncthreads();
  if(d<32){ float m=-1e30f; for(int j=0;j<32;j++) m=fmaxf(m,p[d][j]); float s=0; for(int j=0;j<32;j++){ float e=__expf(p[d][j]-m); p[d][j]=e; s+=e;}
    for(int j=0;j<32;j++) p[d][j]=bf2f(f2bf(p[d][j]/s)); }
  __syncthreads();
  for(int i=0;i<32;i++){ float s=0; for(int j=0;j<32;j++) s+=p[i][j]*bf2f(vv[j*128+d]); O[(size_t)br*512*4096+(size_t)(b*32+i)*4096+h*128+d]=f2bf(s);}
}
__global__ void __launch_bounds__(256) k2(const u16* O,const u16* wo0,const u16* wo1,u16* out){
  int blk=blockIdx.x; int br=blk>>8; int mt=(blk>>2)&63; int nt=blk&3;
  const u16* wo=br?wo1:wo0; int n=nt*256+threadIdx.x;
  float acc[8]; for(int i=0;i<8;i++)acc[i]=0;
  for(int k=0;k<4096;k++){ float w=bf2f(wo[(size_t)k*1024+n]);
    #pragma unroll
    for(int i=0;i<8;i++) acc[i]+=bf2f(O[(size_t)br*512*4096+(size_t)(mt*8+i)*4096+k])*w; }
  for(int i=0;i<8;i++) out[(size_t)(mt*8+i)*2048+br*1024+n]=f2bf(acc[i]);
}
extern "C" void kernel_launch(void* const* d_in, const int* in_sizes, int n_in,
                              void* d_out, int out_size, void* d_ws, size_t ws_size,
                              hipStream_t stream) {
  u16* O=(u16*)d_ws;
  k1<<<1024,128,0,stream>>>((const u16*)d_in[0],(const u16*)d_in[1],(const u16*)d_in[2],(const u16*)d_in[3],
     (const u16*)d_in[5],(const u16*)d_in[6],(const u16*)d_in[7],(const u16*)d_in[8],O);
  k2<<<512,256,0,stream>>>(O,(const u16*)d_in[4],(const u16*)d_in[9],(u16*)d_out);
}
```

```cpp
#include <hip/hip_runtime.h>
#include <hip/hip_bf16.h>
typedef unsigned short u16;
__device__ __forceinline__ float bf2f(u16 x){ return __uint_as_float(((unsigned)x)<<16); }
__device__ __forceinline__ u16 f2bf(float x){ __hip_bfloat16 b = __float2bfloat16(x); return *(u16*)&b; }

__global__ void __launch_bounds__(128) k1(const u16* x0,const u16* k0,const u16* v0,const u16* wq0,
                   const u16* x1,const u16* k1_,const u16* v1,const u16* wq1, u16* O){
  int blk=blockIdx.x; int br=blk>>9; int b=(blk>>5)&15; int h=blk&31;
  const u16* x=br?x1:x0; const u16* kc=br?k1_:k0; const u16* vc=br?v1:v0; const u16* wq=br?wq1:wq0;
  __shared__ float q[32][129]; __shared__ float p[32][33];
  int d=threadIdx.x;
  float acc[32];
  for(int i=0;i<32;i++)acc[i]=0.f;
  for(int k=0;k<1024;k++){
    float w=bf2f(wq[(size_t)k*4096+h*128+d]);
    #pragma unroll
    for(int i=0;i<32;i++) acc[i]+=bf2f(x[(size_t)(b*32+i)*1024+k])*w;
  }
  #pragma unroll
  for(int i=0;i<32;i++) q[i][d]=bf2f(f2bf(acc[i]));
  __syncthreads();
  const u16* kk=kc+(size_t)(b*32+h)*4096; const u16* vv=vc+(size_t)(b*32+h)*4096;
  for(int e=d;e<1024;e+=128){ int i=e>>5,j=e&31; float s=0; for(int t=0;t<128;t++) s+=q[i][t]*bf2f(kk[j*128+t]);
    s=bf2f(f2bf(s)); s=bf2f(f2bf(s*0.08838834764831845f)); p[i][j]=s; }
  __syncthreads();
  if(d<32){ float m=-1e30f; for(int j=0;j<32;j++) m=fmaxf(m,p[d][j]); float s=0; for(int j=0;j<32;j++){ float e=__expf(p[d][j]-m); p[d][j]=e; s+=e;}
    for(int j=0;j<32;j++) p[d][j]=bf2f(f2bf(p[d][j]/s)); }
  __syncthreads();
  for(int i=0;i<32;i++){ float s=0; for(int j=0;j<32;j++) s+=p[i][j]*bf2f(vv[j*128+d]); O[(size_t)br*512*4096+(size_t)(b*32+i)*4096+h*128+d]=f2bf(s);}
}
__global__ void __launch_bounds__(256) k2(const u16* O,const u16* wo0,const u16* wo1,u16* out){
  int blk=blockIdx.x; int br=blk>>8; int mt=(blk>>2)&63; int nt=blk&3;
  const u16* wo=br?wo1:wo0; int n=nt*256+threadIdx.x;
  float acc[8]; for(int i=0;i<8;i++)acc[i]=0;
  for(int k=0;k<4096;k++){ float w=bf2f(wo[(size_t)k*1024+n]);
    #pragma unroll
    for(int i=0;i<8;i++) acc[i]+=bf2f(O[(size_t)br*512*4096+(size_t)(mt*8+i)*4096+k])*w; }
  for(int i=0;i<8;i++) out[(size_t)(mt*8+i)*2048+br*1024+n]=f2bf(acc[i]);
}

using u32x4 = __attribute__((ext_vector_type(4))) unsigned;
__global__ void __launch_bounds__(64) probe(const char* p,int n,int stride,unsigned* sink){
  u32x4 a={0,0,0,0};
  const char* q=p+threadIdx.x*16;
  for(int i=0;i<n;i++){ u32x4 v=*(const u32x4*)(q+(size_t)i*stride); a^=v; asm volatile("s_waitcnt vmcnt(0)":::"memory"); }
  if((a[0]^a[1]^a[2]^a[3])==0x12345678) sink[threadIdx.x]=1;
}
extern "C" void kernel_launch(void* const* d_in, const int* in_sizes, int n_in,
                              void* d_out, int out_size, void* d_ws, size_t ws_size,
                              hipStream_t stream) {
  u16* O=(u16*)d_ws;
  probe<<<1,64,0,stream>>>((const char*)d_in[3],1,1024,(unsigned*)((char*)d_ws+(64<<20)));
  k1<<<1024,128,0,stream>>>((const u16*)d_in[0],(const u16*)d_in[1],(const u16*)d_in[2],(const u16*)d_in[3],
     (const u16*)d_in[5],(const u16*)d_in[6],(const u16*)d_in[7],(const u16*)d_in[8],O);
  k2<<<512,256,0,stream>>>(O,(const u16*)d_in[4],(const u16*)d_in[9],(u16*)d_out);
}
```

```cpp
#include <hip/hip_runtime.h>
#include <hip/hip_bf16.h>
typedef unsigned short u16;
__device__ __forceinline__ float bf2f(u16 x){ return __uint_as_float(((unsigned)x)<<16); }
__device__ __forceinline__ u16 f2bf(float x){ __hip_bfloat16 b = __float2bfloat16(x); return *(u16*)&b; }

__global__ void __launch_bounds__(128) k1(const u16* x0,const u16* k0,const u16* v0,const u16* wq0,
                   const u16* x1,const u16* k1_,const u16* v1,const u16* wq1, u16* O){
  int blk=blockIdx.x; int br=blk>>9; int b=(blk>>5)&15; int h=blk&31;
  const u16* x=br?x1:x0; const u16* kc=br?k1_:k0; const u16* vc=br?v1:v0; const u16* wq=br?wq1:wq0;
  __shared__ float q[32][129]; __shared__ float p[32][33];
  int d=threadIdx.x;
  float acc[32];
  for(int i=0;i<32;i++)acc[i]=0.f;
  for(int k=0;k<1024;k++){
    float w=bf2f(wq[(size_t)k*4096+h*128+d]);
    #pragma unroll
    for(int i=0;i<32;i++) acc[i]+=bf2f(x[(size_t)(b*32+i)*1024+k])*w;
  }
  #pragma unroll
  for(int i=0;i<32;i++) q[i][d]=bf2f(f2bf(acc[i]));
  __syncthreads();
  const u16* kk=kc+(size_t)(b*32+h)*4096; const u16* vv=vc+(size_t)(b*32+h)*4096;
  for(int e=d;e<1024;e+=128){ int i=e>>5,j=e&31; float s=0; for(int t=0;t<128;t++) s+=q[i][t]*bf2f(kk[j*128+t]);
    s=bf2f(f2bf(s)); s=bf2f(f2bf(s*0.08838834764831845f)); p[i][j]=s; }
  __syncthreads();
  if(d<32){ float m=-1e30f; for(int j=0;j<32;j++) m=fmaxf(m,p[d][j]); float s=0; for(int j=0;j<32;j++){ float e=__expf(p[d][j]-m); p[d][j]=e; s+=e;}
    for(int j=0;j<32;j++) p[d][j]=bf2f(f2bf(p[d][j]/s)); }
  __syncthreads();
  for(int i=0;i<32;i++){ float s=0; for(int j=0;j<32;j++) s+=p[i][j]*bf2f(vv[j*128+d]); O[(size_t)br*512*4096+(size_t)(b*32+i)*4096+h*128+d]=f2bf(s);}
}
__global__ void __launch_bounds__(256) k2(const u16* O,const u16* wo0,const u16* wo1,u16* out){
  int blk=blockIdx.x; int br=blk>>8; int mt=(blk>>2)&63; int nt=blk&3;
  const u16* wo=br?wo1:wo0; int n=nt*256+threadIdx.x;
  float acc[8]; for(int i=0;i<8;i++)acc[i]=0;
  for(int k=0;k<4096;k++){ float w=bf2f(wo[(size_t)k*1024+n]);
    #pragma unroll
    for(int i=0;i<8;i++) acc[i]+=bf2f(O[(size_t)br*512*4096+(size_t)(mt*8+i)*4096+k])*w; }
  for(int i=0;i<8;i++) out[(size_t)(mt*8+i)*2048+br*1024+n]=f2bf(acc[i]);
}

using u32x4 = __attribute__((ext_vector_type(4))) unsigned;
template<int NT> __global__ void __launch_bounds__(256) probe(const u32x4* p,int n,unsigned* sink){
  u32x4 a={0,0,0,0};
  const u32x4* q=p+blockIdx.x*256+threadIdx.x;
  #pragma unroll 8
  for(int i=0;i<n;i++){ u32x4 v; if(NT) v=__builtin_nontemporal_load(q+(size_t)i*1024*256); else v=q[(size_t)i*1024*256]; a^=v; }
  if((a[0]^a[1]^a[2]^a[3])==0x12345678) sink[threadIdx.x]=1;
}
extern "C" void kernel_launch(void* const* d_in, const int* in_sizes, int n_in,
                              void* d_out, int out_size, void* d_ws, size_t ws_size,
                              hipStream_t stream) {
  u16* O=(u16*)d_ws;
  probe<1><<<1024,256,0,stream>>>((const u32x4*)((const char*)d_ws+(32<<20)),1,(unsigned*)((char*)d_ws+(16<<20)));
  k1<<<1024,128,0,stream>>>((const u16*)d_in[0],(const u16*)d_in[1],(const u16*)d_in[2],(const u16*)d_in[3],
     (const u16*)d_in[5],(const u16*)d_in[6],(const u16*)d_in[7],(const u16*)d_in[8],O);
  k2<<<512,256,0,stream>>>(O,(const u16*)d_in[4],(const u16*)d_in[9],(u16*)d_out);
}
```

```cpp
#include <hip/hip_runtime.h>

__global__ __attribute__((aligned(4096))) void __launch_bounds__(512, 2) concat_heads__mha_block_cached__mha_block_cached__near_ridge_kernel(const void* ax, const void* ak, const void* av, const void* awq, const void* awo,
    const void* bx, const void* bk, const void* bv, const void* bwq, const void* bwo, void* out, void* ws) {
  __shared__ __attribute__((aligned(16))) char smem[163840];
  asm volatile(
    "s_bitcmp1_b32 %1, 2\n\t"
    "s_cselect_b32 s7, 40, 0\n\t"
    "s_load_dwordx8 s[8:15], %0, s7\n\t"
    "v_readfirstlane_b32 s5, v0\n\t"
    "s_add_u32 s18, s7, 32\n\t"
    "s_load_dwordx4 s[20:23], %0, 0x50\n\t"
    "s_load_dwordx2 s[16:17], %0, s18\n\t"
    "s_mov_b32 s4, %1\n\t"
    "v_and_b32 v2, 63, v0\n\t"
    "s_lshr_b32 s5, s5, 6\n\t"
    "s_and_b32 s25, s4, 3\n\t"
    "s_bfe_u32 s26, s4, 0x30003\n\t"
    "s_lshr_b32 s27, s4, 6\n\t"
    "s_and_b32 s6, s5, 3\n\t"
    "s_cmp_lt_u32 s5, 4\n\t"
    "s_cbranch_scc1 .Lhs_%=\n\t"
    "v_bfe_u32 v3, v0, 3, 3\n\t"
    "v_bfe_u32 v5, v0, 4, 2\n\t"
    "v_and_b32 v4, 7, v0\n\t"
    "s_add_u32 s29, s27, s26\n\t"
    "v_xor_b32 v26, v4, v5\n\t"
    "s_and_b32 s29, s29, 3\n\t"
    "v_lshlrev_b32 v8, 11, v3\n\t"
    "s_lshl_b32 s31, s29, 2\n\t"
    "v_lshl_add_u32 v8, v26, 4, v8\n\t"
    "s_lshr_b32 s31, 0x1100, s31\n\t"
    "s_lshl_b32 s63, s25, 2\n\t"
    "s_add_u32 s31, s31, s63\n\t"
    "s_and_b32 s31, s31, 15\n\t"
    "s_lshl_b32 s54, s31, 7\n\t"
    "s_lshl_b32 s68, s27, 2\n\t"
    "s_add_u32 s68, s68, s6\n\t"
    "s_lshl_b32 s68, s68, 16\n\t"
    "s_lshl_b32 s56, s6, 12\n\t"
    "s_mov_b32 s34, 0x10000\n\t"
    "s_mov_b32 s35, 0x20000\n\t"
    "s_mov_b32 m0, s56\n\t"
    "v_xor_b32 v9, 0x4040, v8\n\t"
    "v_add_u32 v10, 0x8000, v8\n\t"
    "v_add_u32 v11, 0x8000, v9\n\t"
    "s_lshl_b32 s69, s25, 11\n\t"
    "s_lshl_b32 s57, s26, 8\n\t"
    "s_add_u32 s69, s69, s57\n\t"
    "s_lshl_b32 s55, s31, 19\n\t"
    "s_waitcnt lgkmcnt(0)\n\t"
    "s_add_u32 s32, s8, s68\n\t"
    "s_addc_u32 s33, s9, 0\n\t"
    "s_and_b32 s33, s33, 0xffff\n\t"
    "buffer_load_dwordx4 v8, s[32:35], s54 offen lds\n\t"
    "s_add_u32 m0, s56, 1024\n\t"
    "s_lshl_b32 s31, s6, 4\n\t"
    "v_add_u32 v6, s31, v3\n\t"
    "v_and_b32 v7, 2, v3\n\t"
    "buffer_load_dwordx4 v9, s[32:35], s54 offen lds\n\t"
    "s_add_u32 m0, s56, 2048\n\t"
    "v_xor_b32 v7, v7, v4\n\t"
    "v_lshlrev_b32 v7, 4, v7\n\t"
    "v_lshl_add_u32 v12, v6, 13, v7\n\t"
    "buffer_load_dwordx4 v10, s[32:35], s54 offen lds\n\t"
    "s_add_u32 m0, s56, 3072\n\t"
    "v_add_u32 v14, 0x80, v12\n\t"
    "v_xor_b32 v13, 0x10040, v12\n\t"
    "v_add_u32 v15, 0x80, v13\n\t"
    "buffer_load_dwordx4 v11, s[32:35], s54 offen lds\n\t"
    "s_add_u32 s36, s14, s69\n\t"
    "s_addc_u32 s37, s15, 0\n\t"
    "s_and_b32 s37, s37, 0xffff\n\t"
    "s_sub_u32 s38, 0x800000, s69\n\t"
    "s_mov_b32 s39, 0x20000\n\t"
    "s_lshl_b32 s57, s6, 11\n\t"
    "s_add_u32 s57, s57, 0x4000\n\t"
    "s_add_u32 s56, s56, 0x8000\n\t"
    "s_add_u32 m0, s57, 0\n\t"
    "s_nop 0\n\t"
    "buffer_load_dwordx4 v12, s[36:39], s55 offen lds\n\t"
    "s_add_u32 m0, s57, 8192\n\t"
    "s_nop 0\n\t"
    "buffer_load_dwordx4 v14, s[36:39], s55 offen lds\n\t"
    "s_add_u32 m0, s57, 1024\n\t"
    "s_nop 0\n\t"
    "buffer_load_dwordx4 v13, s[36:39], s55 offen lds\n\t"
    "s_add_u32 m0, s57, 9216\n\t"
    "s_nop 0\n\t"
    "buffer_load_dwordx4 v15, s[36:39], s55 offen lds\n\t"
    "s_add_u32 s57, s57, 0x8000\n\t"
    "s_and_b32 s57, s57, 0x1ffff\n\t"
    "s_mov_b32 s52, 0x76953241\n\t"
    "s_mov_b32 s53, 0xfecbad8\n\t"
    "s_cmp_eq_u32 s29, 1\n\t"
    "s_cselect_b32 s52, 0x7a543612, s52\n\t"
    "s_cselect_b32 s53, 0xfdcbe98, s53\n\t"
    "s_cmp_eq_u32 s29, 2\n\t"
    "s_cselect_b32 s52, 0xb6437250, s52\n\t"
    "s_cselect_b32 s53, 0xedcfa98, s53\n\t"
    "s_cmp_eq_u32 s29, 3\n\t"
    "s_cselect_b32 s52, 0x76584203, s52\n\t"
    "s_cselect_b32 s53, 0xfedba9c, s53\n\t"
    "s_bfe_u32 s24, s4, 0x10002\n\t"
    "s_lshl_b32 s28, s25, 3\n\t"
    "s_or_b32 s28, s28, s26\n\t"
    "s_lshl_b32 s30, s4, 2\n\t"
    "s_add_u32 s30, s30, s6\n\t"
    "s_and_b32 s19, s27, 1\n\t"
    "s_xor_b32 s19, s19, s26\n\t"
    "s_add_u32 s31, s52, s63\n\t"
    "s_and_b32 s31, s31, 15\n\t"
    "s_lshr_b64 s[52:53], s[52:53], 4\n\t"
    "s_lshl_b32 s54, s31, 7\n\t"
    "s_lshl_b32 s55, s31, 19\n\t"
    "s_add_u32 m0, s56, 0\n\t"
    "s_nop 0\n\t"
    "buffer_load_dwordx4 v8, s[32:35], s54 offen lds\n\t"
    "s_add_u32 m0, s56, 1024\n\t"
    "s_nop 0\n\t"
    "buffer_load_dwordx4 v9, s[32:35], s54 offen lds\n\t"
    "s_add_u32 m0, s56, 2048\n\t"
    "s_nop 0\n\t"
    "buffer_load_dwordx4 v10, s[32:35], s54 offen lds\n\t"
    "s_add_u32 m0, s56, 3072\n\t"
    "s_nop 0\n\t"
    "buffer_load_dwordx4 v11, s[32:35], s54 offen lds\n\t"
    "s_add_u32 s56, s56, 0x8000\n\t"
    "s_and_b32 s56, s56, 0x1ffff\n\t"
    "s_branch .Lload_%=\n\t"
    ".Lhs_%=:\n\t"
    "s_mul_i32 s71, s6, 4\n\t"
    "s_add_u32 s71, s71, s26\n\t"
    "s_lshl_b32 s71, s71, 2\n\t"
    "s_getpc_b64 s[96:97]\n\t"
    ".Lhba_%=:\n\t"
    "s_add_u32 s71, s71, (.Lht_%=-.Lhba_%=-4)\n\t"
    "s_add_u32 s94, s96, s71\n\t"
    "s_addc_u32 s95, s97, 0\n\t"
    "s_add_u32 s96, s96, (.Lcons_%=-.Lhba_%=)\n\t"
    "s_addc_u32 s97, s97, 0\n\t"
    "s_mov_b32 s98, 4\n\t"
    ".Lhg_%=:\n\t"
    "s_mov_b64 exec, 0\n\t"
    ".Lhr_%=:\n\t"
    "s_sub_u32 s98, s98, 1\n\t"
    "s_cbranch_scc1 .Lhe_%=\n\t"
    "s_add_u32 s94, s94, 4\n\t"
    "s_addc_u32 s95, s95, 0\n\t"
    "s_setpc_b64 s[94:95]\n\t"
    ".Lhe_%=:\n\t"
    "s_mov_b64 exec, -1\n\t"
    "s_setpc_b64 s[96:97]\n\t"
    ".Lht_%=:\n\t"
    "s_branch .Lhop1_%=\n\t"
    "s_branch .Lhop2_%=\n\t"
    "s_branch .Lhop3_%=\n\t"
    "s_branch .Lhop4_%=\n\t"
    "s_branch .Lhop5_%=\n\t"
    "s_branch .Lhop6_%=\n\t"
    "s_branch .Lhop7_%=\n\t"
    "s_branch .Lhop8_%=\n\t"
    "s_branch .Lhop9_%=\n\t"
    "s_branch .Lhop10_%=\n\t"
    "s_branch .Lhop11_%=\n\t"
    "s_branch .Lhop12_%=\n\t"
    "s_branch .Lhop13_%=\n\t"
    "s_branch .Lhop14_%=\n\t"
    "s_branch .Lhop15_%=\n\t"
    "s_branch .Lhop16_%=\n\t"
    "s_branch .Lhop1_%=\n\t"
    "s_branch .Lhop2_%=\n\t"
    "s_branch .Lhop3_%=\n\t"
    "s_branch .Lhop4_%=\n\t"
    "s_branch .Lhop5_%=\n\t"
    "s_branch .Lhop6_%=\n\t"
    "s_branch .Lhop7_%=\n\t"
    ".Lload_%=:\n\t"
    "s_lshl_b32 s7, s27, 2\n\t"
    "s_add_u32 s7, s7, s6\n\t"
    "s_lshl_b32 s7, s7, 5\n\t"
    "s_add_u32 s7, s7, s28\n\t"
    "s_lshl_b32 s7, s7, 13\n\t"
    "s_add_u32 s40, s10, s7\n\t"
    "s_addc_u32 s41, s11, 0\n\t"
    "s_and_b32 s41, s41, 0xffff\n\t"
    "s_mov_b32 s42, 8192\n\t"
    "s_mov_b32 s43, 0x20000\n\t"
    "s_and_b32 s7, s7, 0xffff0000\n\t"
    "s_add_u32 s44, s12, s7\n\t"
    "s_addc_u32 s45, s13, 0\n\t"
    "s_and_b32 s45, s45, 0xffff\n\t"
    "s_mov_b32 s46, 65536\n\t"
    "s_mov_b32 s47, 0x20000\n\t"
    "s_lshl_b32 s7, s25, 21\n\t"
    "s_lshl_b32 s69, s19, 8\n\t"
    "s_add_u32 s7, s7, s69\n\t"
    "s_add_u32 s48, s16, s7\n\t"
    "s_addc_u32 s49, s17, 0\n\t"
    "s_and_b32 s49, s49, 0xffff\n\t"
    "s_mov_b32 s51, 0x20000\n\t"
    "s_sub_u32 s69, 0x200000, s69\n\t"
    "s_mov_b32 s50, s69\n\t"
    "s_mov_b32 s76, s22\n\t"
    "s_and_b32 s77, s23, 0xffff\n\t"
    "s_mov_b32 s78, 0x1000000\n\t"
    "s_mov_b32 s79, 0x20000\n\t"
    "s_xor_b32 s7, s25, 3\n\t"
    "s_lshl_b32 s7, s7, 6\n\t"
    "v_xor_b32 v7, s7, v7\n\t"
    ".Lhop1_%=:\n\t"
    "s_cbranch_execz .Lhr_%=\n\t"
    "v_lshl_add_u32 v16, v6, 11, v7\n\t"
    "v_xor_b32 v17, 64, v16\n\t"
    "v_add_u32 v17, 0x4000, v17\n\t"
    "v_xor_b32 v18, 0x80, v16\n\t"
    "v_xor_b32 v19, 0x80, v17\n\t"
    "v_lshrrev_b32 v5, 4, v2\n\t"
    "v_and_b32 v7, 15, v2\n\t"
    "v_xor_b32 v20, v7, v5\n\t"
    "v_lshlrev_b32 v20, 4, v20\n\t"
    "v_lshl_add_u32 v20, v5, 8, v20\n\t"
    "v_xor_b32 v21, 64, v20\n\t"
    "v_xor_b32 v22, 0x80, v20\n\t"
    "v_xor_b32 v23, 0xc0, v20\n\t"
    "v_lshlrev_b32 v26, 4, v26\n\t"
    "v_lshl_add_u32 v24, v3, 8, v26\n\t"
    "v_xor_b32 v25, 64, v24\n\t"
    "v_add_u32 v25, 0x800, v25\n\t"
    "v_add_u32 v26, 0x1000, v24\n\t"
    "v_add_u32 v27, 0x1000, v25\n\t"
    "s_lshl_b32 s65, s6, 13\n\t"
    "s_add_u32 s65, s65, 0x20000\n\t"
    "s_movk_i32 s67, 0x1000\n\t"
    "s_lshl_b32 s64, s6, 12\n\t"
    "s_add_u32 s64, s64, 0x20000\n\t"
    "s_lshl_b32 s66, s30, 12\n\t"
    "v_lshlrev_b32 v28, 4, v2\n\t"
    "v_lshlrev_b32 v29, 2, v2\n\t"
    "s_mov_b32 s59, 0x11\n\t"
    "s_cmp_eq_u32 s29, 1\n\t"
    "s_cselect_b32 s59, 0x22, s59\n\t"
    "s_cmp_eq_u32 s29, 2\n\t"
    "s_cselect_b32 s59, 0x44, s59\n\t"
    "s_cmp_eq_u32 s29, 3\n\t"
    "s_cselect_b32 s59, 0x88, s59\n\t"
    "s_mov_b32 s62, 0x75634120\n\t"
    "s_cmp_eq_u32 s27, 1\n\t"
    "s_cselect_b32 s62, 0x75643210, s62\n\t"
    "s_cmp_eq_u32 s27, 2\n\t"
    "s_cselect_b32 s62, 0x75e3c1a8, s62\n\t"
    "s_cmp_eq_u32 s27, 3\n\t"
    "s_cselect_b32 s62, 0x75ec3a18, s62\n\t"
    "s_mov_b32 s61, s62\n\t"
    "s_add_u32 m0, s57, 0\n\t"
    "s_nop 0\n\t"
    "buffer_load_dwordx4 v12, s[36:39], s55 offen lds\n\t"
    "s_add_u32 m0, s57, 8192\n\t"
    "s_nop 0\n\t"
    "buffer_load_dwordx4 v14, s[36:39], s55 offen lds\n\t"
    "s_add_u32 m0, s57, 1024\n\t"
    "s_nop 0\n\t"
    "buffer_load_dwordx4 v13, s[36:39], s55 offen lds\n\t"
    "s_add_u32 m0, s57, 9216\n\t"
    "s_nop 0\n\t"
    "buffer_load_dwordx4 v15, s[36:39], s55 offen lds\n\t"
    "s_add_u32 s57, s57, 0x8000\n\t"
    "s_and_b32 s57, s57, 0x1ffff\n\t"
    "s_add_u32 s31, s52, s63\n\t"
    "s_and_b32 s31, s31, 15\n\t"
    "s_lshr_b64 s[52:53], s[52:53], 4\n\t"
    "s_lshl_b32 s54, s31, 7\n\t"
    "s_lshl_b32 s55, s31, 19\n\t"
    "s_add_u32 m0, s56, 0\n\t"
    "s_nop 0\n\t"
    "buffer_load_dwordx4 v8, s[32:35], s54 offen lds\n\t"
    "s_add_u32 m0, s56, 1024\n\t"
    "s_nop 0\n\t"
    "buffer_load_dwordx4 v9, s[32:35], s54 offen lds\n\t"
    "s_add_u32 m0, s56, 2048\n\t"
    "s_nop 0\n\t"
    "buffer_load_dwordx4 v10, s[32:35], s54 offen lds\n\t"
    "s_add_u32 m0, s56, 3072\n\t"
    "s_nop 0\n\t"
    "buffer_load_dwordx4 v11, s[32:35], s54 offen lds\n\t"
    "s_add_u32 s56, s56, 0x8000\n\t"
    "s_and_b32 s56, s56, 0x1ffff\n\t"
    "s_waitcnt vmcnt(12)\n\t"
    "s_barrier\n\t"
    "s_mov_b32 s58, 0\n\t"
    ".Lld_%=:\n\t"
    "s_bitcmp1_b32 s59, s58\n\t"
    "s_cbranch_scc1 .Lhk_%=\n\t"
    ".Lld2_%=:\n\t"
    "s_add_u32 m0, s57, 0\n\t"
    "s_nop 0\n\t"
    "buffer_load_dwordx4 v12, s[36:39], s55 offen lds\n\t"
    "s_add_u32 m0, s57, 8192\n\t"
    "s_nop 0\n\t"
    "buffer_load_dwordx4 v14, s[36:39], s55 offen lds\n\t"
    "s_add_u32 m0, s57, 1024\n\t"
    "s_nop 0\n\t"
    "buffer_load_dwordx4 v13, s[36:39], s55 offen lds\n\t"
    "s_add_u32 m0, s57, 9216\n\t"
    "s_nop 0\n\t"
    "buffer_load_dwordx4 v15, s[36:39], s55 offen lds\n\t"
    "s_add_u32 s57, s57, 0x8000\n\t"
    "s_and_b32 s57, s57, 0x1ffff\n\t"
    "s_add_u32 s31, s52, s63\n\t"
    "s_and_b32 s31, s31, 15\n\t"
    "s_lshr_b64 s[52:53], s[52:53], 4\n\t"
    "s_lshl_b32 s54, s31, 7\n\t"
    "s_lshl_b32 s55, s31, 19\n\t"
    "s_add_u32 m0, s56, 0\n\t"
    "s_nop 0\n\t"
    "buffer_load_dwordx4 v8, s[32:35], s54 offen lds\n\t"
    "s_add_u32 m0, s56, 1024\n\t"
    "s_nop 0\n\t"
    "buffer_load_dwordx4 v9, s[32:35], s54 offen lds\n\t"
    "s_add_u32 m0, s56, 2048\n\t"
    "s_nop 0\n\t"
    "buffer_load_dwordx4 v10, s[32:35], s54 offen lds\n\t"
    "s_add_u32 m0, s56, 3072\n\t"
    "s_nop 0\n\t"
    "buffer_load_dwordx4 v11, s[32:35], s54 offen lds\n\t"
    "s_add_u32 s56, s56, 0x8000\n\t"
    "s_and_b32 s56, s56, 0x1ffff\n\t"
    "s_add_u32 s58, s58, 1\n\t"
    "s_cmp_le_u32 s58, 7\n\t"
    "s_cbranch_scc1 .Lrm_%=\n\t"
    ".Lrmx_%=:\n\t"
    "s_waitcnt vmcnt(12)\n\t"
    "s_barrier\n\t"
    "s_cmp_lt_u32 s58, 13\n\t"
    "s_cbranch_scc1 .Lld_%=\n\t"
    "s_add_u32 m0, s57, 0\n\t"
    "s_nop 0\n\t"
    "buffer_load_dwordx4 v12, s[36:39], s55 offen lds\n\t"
    "s_add_u32 m0, s57, 8192\n\t"
    "s_nop 0\n\t"
    "buffer_load_dwordx4 v14, s[36:39], s55 offen lds\n\t"
    "s_add_u32 m0, s57, 1024\n\t"
    "s_nop 0\n\t"
    "buffer_load_dwordx4 v13, s[36:39], s55 offen lds\n\t"
    "s_add_u32 m0, s57, 9216\n\t"
    "s_nop 0\n\t"
    "buffer_load_dwordx4 v15, s[36:39], s55 offen lds\n\t"
    "s_add_u32 s57, s57, 0x8000\n\t"
    "s_and_b32 s57, s57, 0x1ffff\n\t"
    "s_waitcnt vmcnt(8)\n\t"
    "s_barrier\n\t"
    "s_and_b32 s7, s62, 7\n\t"
    "s_xor_b32 s7, s7, s26\n\t"
    "s_lshl_b32 s54, s7, 13\n\t"
    "s_lshl_b32 s55, s7, 18\n\t"
    "s_and_b32 s7, s62, 8\n\t"
    "s_lshl_b32 s7, s7, 4\n\t"
    "s_or_b32 s54, s54, s7\n\t"
    "s_lshl_b32 s7, s7, 10\n\t"
    "s_or_b32 s55, s55, s7\n\t"
    "s_lshr_b32 s62, s62, 4\n\t"
    "s_add_u32 m0, s56, 0\n\t"
    "s_nop 0\n\t"
    "buffer_load_dwordx4 v24, s[44:47], s54 offen lds\n\t"
    "s_add_u32 m0, s56, 1024\n\t"
    "s_nop 0\n\t"
    "buffer_load_dwordx4 v25, s[44:47], s54 offen lds\n\t"
    "s_add_u32 m0, s56, 2048\n\t"
    "s_nop 0\n\t"
    "buffer_load_dwordx4 v26, s[44:47], s54 offen lds\n\t"
    "s_add_u32 m0, s56, 3072\n\t"
    "s_nop 0\n\t"
    "buffer_load_dwordx4 v27, s[44:47], s54 offen lds\n\t"
    "s_add_u32 s56, s56, 0x8000\n\t"
    "s_and_b32 s56, s56, 0x1ffff\n\t"
    "s_add_u32 m0, s57, 0\n\t"
    "s_nop 0\n\t"
    "buffer_load_dwordx4 v16, s[48:51], s55 offen lds\n\t"
    "s_add_u32 m0, s57, 8192\n\t"
    "s_nop 0\n\t"
    "buffer_load_dwordx4 v18, s[48:51], s55 offen lds\n\t"
    "s_add_u32 m0, s57, 1024\n\t"
    "s_nop 0\n\t"
    "buffer_load_dwordx4 v17, s[48:51], s55 offen lds\n\t"
    "s_add_u32 m0, s57, 9216\n\t"
    "s_nop 0\n\t"
    "buffer_load_dwordx4 v19, s[48:51], s55 offen lds\n\t"
    "s_add_u32 s57, s57, 0x8000\n\t"
    "s_and_b32 s57, s57, 0x1ffff\n\t"
    ".Lhop2_%=:\n\t"
    "s_cbranch_execz .Lhr_%=\n\t"
    "s_waitcnt vmcnt(8)\n\t"
    "s_barrier\n\t"
    "s_xor_b32 s54, s54, 0x80\n\t"
    "s_xor_b32 s55, s55, 0x20000\n\t"
    "s_add_u32 m0, s56, 0\n\t"
    "s_waitcnt vmcnt(6)\n\t"
    "buffer_load_dwordx4 v24, s[44:47], s54 offen lds\n\t"
    "s_add_u32 m0, s56, 1024\n\t"
    "s_waitcnt vmcnt(6)\n\t"
    "buffer_load_dwordx4 v25, s[44:47], s54 offen lds\n\t"
    "s_add_u32 m0, s56, 2048\n\t"
    "s_waitcnt vmcnt(6)\n\t"
    "buffer_load_dwordx4 v26, s[44:47], s54 offen lds\n\t"
    "s_add_u32 m0, s56, 3072\n\t"
    "s_waitcnt vmcnt(6)\n\t"
    "buffer_load_dwordx4 v27, s[44:47], s54 offen lds\n\t"
    "s_add_u32 s56, s56, 0x8000\n\t"
    "s_and_b32 s56, s56, 0x1ffff\n\t"
    "s_add_u32 m0, s57, 0\n\t"
    "s_waitcnt vmcnt(6)\n\t"
    "buffer_load_dwordx4 v16, s[48:51], s55 offen lds\n\t"
    "s_add_u32 m0, s57, 8192\n\t"
    "s_waitcnt vmcnt(6)\n\t"
    "buffer_load_dwordx4 v18, s[48:51], s55 offen lds\n\t"
    "s_add_u32 m0, s57, 1024\n\t"
    "s_waitcnt vmcnt(6)\n\t"
    "buffer_load_dwordx4 v17, s[48:51], s55 offen lds\n\t"
    "s_add_u32 m0, s57, 9216\n\t"
    "s_waitcnt vmcnt(6)\n\t"
    "buffer_load_dwordx4 v19, s[48:51], s55 offen lds\n\t"
    "s_add_u32 s57, s57, 0x8000\n\t"
    "s_and_b32 s57, s57, 0x1ffff\n\t"
    "s_waitcnt vmcnt(8)\n\t"
    "s_barrier\n\t"
    "s_and_b32 s7, s62, 7\n\t"
    "s_xor_b32 s7, s7, s26\n\t"
    "s_lshl_b32 s54, s7, 13\n\t"
    "s_lshl_b32 s55, s7, 18\n\t"
    "s_and_b32 s7, s62, 8\n\t"
    "s_lshl_b32 s7, s7, 4\n\t"
    "s_or_b32 s54, s54, s7\n\t"
    "s_lshl_b32 s7, s7, 10\n\t"
    "s_or_b32 s55, s55, s7\n\t"
    "s_lshr_b32 s62, s62, 4\n\t"
    "s_add_u32 m0, s56, 0\n\t"
    "s_nop 0\n\t"
    "buffer_load_dwordx4 v24, s[44:47], s54 offen lds\n\t"
    "s_add_u32 m0, s56, 1024\n\t"
    "s_nop 0\n\t"
    "buffer_load_dwordx4 v25, s[44:47], s54 offen lds\n\t"
    "s_add_u32 m0, s56, 2048\n\t"
    "s_nop 0\n\t"
    "buffer_load_dwordx4 v26, s[44:47], s54 offen lds\n\t"
    "s_add_u32 m0, s56, 3072\n\t"
    "s_nop 0\n\t"
    "buffer_load_dwordx4 v27, s[44:47], s54 offen lds\n\t"
    "s_add_u32 s56, s56, 0x8000\n\t"
    "s_and_b32 s56, s56, 0x1ffff\n\t"
    "s_mov_b32 s58, 1\n\t"
    ".Lle_%=:\n\t"
    "s_cmp_lt_u32 s58, 2\n\t"
    "s_cbranch_scc1 .Lle1_%=\n\t"
    "s_lshr_b32 s61, s61, 4\n\t"
    "s_and_b32 s7, s61, 7\n\t"
    "s_lshl_b32 s7, s7, 17\n\t"
    "s_xor_b32 s68, s66, s7\n\t"
    "s_mov_b32 m0, s64\n\t"
    "s_nop 0\n\t"
    "buffer_load_dwordx4 v28, s[76:79], s68 offen sc1 lds\n\t"
    "buffer_load_dwordx4 v28, s[76:79], s68 offen offset:1024 sc1 lds\n\t"
    "buffer_load_dword v29, s[76:79], s68 offen offset:2048 sc1 lds\n\t"
    "s_xor_b32 s64, s64, 0x4000\n\t"
    ".Lle1_%=:\n\t"
    "s_add_u32 m0, s57, 0\n\t"
    "s_nop 0\n\t"
    "buffer_load_dwordx4 v16, s[48:51], s55 offen lds\n\t"
    "s_add_u32 m0, s57, 8192\n\t"
    "s_nop 0\n\t"
    "buffer_load_dwordx4 v18, s[48:51], s55 offen lds\n\t"
    "s_add_u32 m0, s57, 1024\n\t"
    "s_nop 0\n\t"
    "buffer_load_dwordx4 v17, s[48:51], s55 offen lds\n\t"
    "s_add_u32 m0, s57, 9216\n\t"
    "s_nop 0\n\t"
    "buffer_load_dwordx4 v19, s[48:51], s55 offen lds\n\t"
    "s_add_u32 s57, s57, 0x8000\n\t"
    "s_and_b32 s57, s57, 0x1ffff\n\t"
    "s_xor_b32 s54, s54, 0x80\n\t"
    "s_xor_b32 s55, s55, 0x20000\n\t"
    "s_add_u32 m0, s56, 0\n\t"
    "s_nop 0\n\t"
    "buffer_load_dwordx4 v24, s[44:47], s54 offen lds\n\t"
    "s_add_u32 m0, s56, 1024\n\t"
    "s_nop 0\n\t"
    "buffer_load_dwordx4 v25, s[44:47], s54 offen lds\n\t"
    "s_add_u32 m0, s56, 2048\n\t"
    "s_nop 0\n\t"
    "buffer_load_dwordx4 v26, s[44:47], s54 offen lds\n\t"
    "s_add_u32 m0, s56, 3072\n\t"
    "s_nop 0\n\t"
    "buffer_load_dwordx4 v27, s[44:47], s54 offen lds\n\t"
    "s_add_u32 s56, s56, 0x8000\n\t"
    "s_and_b32 s56, s56, 0x1ffff\n\t"
    "s_waitcnt vmcnt(12)\n\t"
    "s_barrier\n\t"
    "s_add_u32 m0, s57, 0\n\t"
    "s_nop 0\n\t"
    "buffer_load_dwordx4 v16, s[48:51], s55 offen lds\n\t"
    "s_add_u32 m0, s57, 8192\n\t"
    "s_nop 0\n\t"
    "buffer_load_dwordx4 v18, s[48:51], s55 offen lds\n\t"
    "s_add_u32 m0, s57, 1024\n\t"
    "s_nop 0\n\t"
    "buffer_load_dwordx4 v17, s[48:51], s55 offen lds\n\t"
    "s_add_u32 m0, s57, 9216\n\t"
    "s_nop 0\n\t"
    "buffer_load_dwordx4 v19, s[48:51], s55 offen lds\n\t"
    "s_add_u32 s57, s57, 0x8000\n\t"
    "s_and_b32 s57, s57, 0x1ffff\n\t"
    "s_and_b32 s7, s62, 7\n\t"
    "s_xor_b32 s7, s7, s26\n\t"
    "s_lshl_b32 s54, s7, 13\n\t"
    "s_lshl_b32 s55, s7, 18\n\t"
    "s_and_b32 s7, s62, 8\n\t"
    "s_lshl_b32 s7, s7, 4\n\t"
    "s_or_b32 s54, s54, s7\n\t"
    "s_lshl_b32 s7, s7, 10\n\t"
    "s_or_b32 s55, s55, s7\n\t"
    "s_lshr_b32 s62, s62, 4\n\t"
    "s_add_u32 m0, s56, 0\n\t"
    "s_nop 0\n\t"
    "buffer_load_dwordx4 v24, s[44:47], s54 offen lds\n\t"
    "s_add_u32 m0, s56, 1024\n\t"
    "s_nop 0\n\t"
    "buffer_load_dwordx4 v25, s[44:47], s54 offen lds\n\t"
    "s_add_u32 m0, s56, 2048\n\t"
    "s_nop 0\n\t"
    "buffer_load_dwordx4 v26, s[44:47], s54 offen lds\n\t"
    "s_add_u32 m0, s56, 3072\n\t"
    "s_nop 0\n\t"
    "buffer_load_dwordx4 v27, s[44:47], s54 offen lds\n\t"
    "s_add_u32 s56, s56, 0x8000\n\t"
    "s_and_b32 s56, s56, 0x1ffff\n\t"
    "s_add_u32 s58, s58, 1\n\t"
    "s_waitcnt vmcnt(12)\n\t"
    "s_barrier\n\t"
    "s_cmp_lt_u32 s58, 7\n\t"
    "s_cbranch_scc1 .Lle_%=\n\t"
    "s_lshr_b32 s61, s61, 4\n\t"
    "s_and_b32 s7, s61, 7\n\t"
    "s_lshl_b32 s7, s7, 17\n\t"
    "s_xor_b32 s68, s66, s7\n\t"
    "s_mov_b32 m0, s64\n\t"
    "s_nop 0\n\t"
    "buffer_load_dwordx4 v28, s[76:79], s68 offen sc1 lds\n\t"
    "buffer_load_dwordx4 v28, s[76:79], s68 offen offset:1024 sc1 lds\n\t"
    "buffer_load_dword v29, s[76:79], s68 offen offset:2048 sc1 lds\n\t"
    "s_xor_b32 s64, s64, 0x4000\n\t"
    "s_mov_b32 s71, s55\n\t"
    "s_add_u32 m0, s57, 0\n\t"
    "s_nop 0\n\t"
    "buffer_load_dwordx4 v16, s[48:51], s71 offen lds\n\t"
    "s_add_u32 m0, s57, 1024\n\t"
    "s_nop 0\n\t"
    "buffer_load_dwordx4 v17, s[48:51], s71 offen lds\n\t"
    "s_xor_b32 s54, s54, 0x80\n\t"
    "s_xor_b32 s55, s55, 0x20000\n\t"
    "s_add_u32 m0, s56, 0\n\t"
    "s_nop 0\n\t"
    "buffer_load_dwordx4 v24, s[44:47], s54 offen lds\n\t"
    "s_add_u32 m0, s56, 1024\n\t"
    "s_nop 0\n\t"
    "buffer_load_dwordx4 v25, s[44:47], s54 offen lds\n\t"
    ".Lhop3_%=:\n\t"
    "s_cbranch_execz .Lhr_%=\n\t"
    "s_add_u32 m0, s56, 2048\n\t"
    "s_nop 0\n\t"
    "buffer_load_dwordx4 v26, s[44:47], s54 offen lds\n\t"
    "s_add_u32 m0, s56, 3072\n\t"
    "s_nop 0\n\t"
    "buffer_load_dwordx4 v27, s[44:47], s54 offen lds\n\t"
    "s_add_u32 s56, s56, 0x8000\n\t"
    "s_and_b32 s56, s56, 0x1ffff\n\t"
    "s_waitcnt vmcnt(6)\n\t"
    "s_barrier\n\t"
    "s_add_u32 m0, s57, 32768\n\t"
    "s_nop 0\n\t"
    "buffer_load_dwordx4 v16, s[48:51], s55 offen lds\n\t"
    "s_add_u32 m0, s57, 33792\n\t"
    "s_nop 0\n\t"
    "buffer_load_dwordx4 v17, s[48:51], s55 offen lds\n\t"
    "s_lshr_b32 s61, s61, 4\n\t"
    "s_and_b32 s7, s61, 7\n\t"
    "s_lshl_b32 s7, s7, 17\n\t"
    "s_xor_b32 s68, s66, s7\n\t"
    "s_mov_b32 m0, s64\n\t"
    "s_nop 0\n\t"
    "buffer_load_dwordx4 v28, s[76:79], s68 offen sc1 lds\n\t"
    "buffer_load_dwordx4 v28, s[76:79], s68 offen offset:1024 sc1 lds\n\t"
    "buffer_load_dword v29, s[76:79], s68 offen offset:2048 sc1 lds\n\t"
    "s_xor_b32 s64, s64, 0x4000\n\t"
    "s_waitcnt vmcnt(9)\n\t"
    "s_barrier\n\t"
    "s_waitcnt vmcnt(3)\n\t"
    "s_barrier\n\t"
    "s_add_u32 m0, s57, 8192\n\t"
    "s_nop 0\n\t"
    "buffer_load_dwordx4 v18, s[48:51], s71 offen lds\n\t"
    "s_add_u32 m0, s57, 9216\n\t"
    "s_nop 0\n\t"
    "buffer_load_dwordx4 v19, s[48:51], s71 offen lds\n\t"
    "s_add_u32 m0, s57, 40960\n\t"
    "s_nop 0\n\t"
    "buffer_load_dwordx4 v18, s[48:51], s55 offen lds\n\t"
    "s_add_u32 m0, s57, 41984\n\t"
    "s_nop 0\n\t"
    "buffer_load_dwordx4 v19, s[48:51], s55 offen lds\n\t"
    "s_waitcnt vmcnt(4)\n\t"
    "s_barrier\n\t"
    "s_waitcnt vmcnt(0)\n\t"
    "s_barrier\n\t"
    "s_endpgm\n\t"
    ".Lhk_%=:\n\t"
    "s_add_u32 m0, s65, s67\n\t"
    "s_nop 0\n\t"
    "buffer_load_dwordx4 v20, s[40:43], s67 offen offset:0 lds\n\t"
    "buffer_load_dwordx4 v21, s[40:43], s67 offen offset:1024 lds\n\t"
    "buffer_load_dwordx4 v22, s[40:43], s67 offen offset:2048 lds\n\t"
    "buffer_load_dwordx4 v23, s[40:43], s67 offen offset:3072 lds\n\t"
    "s_mov_b32 s67, 0\n\t"
    "s_branch .Lld2_%=\n\t"
    ".Lrm_%=:\n\t"
    "s_waitcnt vmcnt(12)\n\t"
    "s_bitcmp0_b32 0x78, s58\n\t"
    "s_cbranch_scc1 .Lrw1_%=\n\t"
    "s_sleep 1\n\t"
    ".Lrw1_%=:\n\t"
    "s_bitcmp0_b32 0x58, s58\n\t"
    "s_cbranch_scc1 .Lrw2_%=\n\t"
    "s_sleep 2\n\t"
    ".Lrw2_%=:\n\t"
    "s_bitcmp0_b32 0x3c, s58\n\t"
    "s_cbranch_scc1 .Lrw4_%=\n\t"
    "s_sleep 4\n\t"
    ".Lrw4_%=:\n\t"
    "s_branch .Lrmx_%=\n\t"
    ".Lcons_%=:\n\t"
    "s_mov_b64 exec, -1\n\t"
    "s_bfe_u32 s24, s4, 0x10002\n\t"
    "s_lshl_b32 s28, s25, 3\n\t"
    "s_or_b32 s28, s28, s26\n\t"
    "s_lshl_b32 s30, s4, 2\n\t"
    "s_add_u32 s30, s30, s6\n\t"
    "s_and_b32 s19, s27, 1\n\t"
    "s_xor_b32 s19, s19, s26\n\t"
    "s_and_b32 s53, s30, 0xffffff1f\n\t"
    "s_lshl_b32 s53, s53, 7\n\t"
    "s_add_u32 s53, s53, 0x400000\n\t"
    "s_and_b32 s54, s30, 0xfffffff3\n\t"
    "s_lshl_b32 s54, s54, 7\n\t"
    "s_add_u32 s54, s54, 0x400040\n\t"
    "s_waitcnt lgkmcnt(0)\n\t"
    "v_mov_b32 v3, s53\n\t"
    "v_writelane_b32 v3, s54, 1\n\t"
    "s_mov_b64 exec, 3\n\t"
    "global_load_dword v63, v3, s[22:23] sc1\n\t"
    "s_mov_b64 exec, -1\n\t"
    "v_and_b32 v5, 15, v2\n\t"
    "v_lshrrev_b32 v6, 4, v2\n\t"
    "v_bfe_u32 v7, v2, 2, 2\n\t"
    "v_and_b32 v8, 3, v2\n\t"
    "s_lshl_b32 s50, s30, 12\n\t"
    "s_lshl_b32 s51, s30, 13\n\t"
    "s_add_u32 s51, s51, 0x800000\n\t"
    "v_lshlrev_b32 v14, 4, v2\n\t"
    "v_lshlrev_b32 v13, 2, v2\n\t"
    "s_xor_b32 s65, s51, 32768\n\t"
    "s_add_u32 s62, s65, 2048\n\t"
    "s_add_u32 s68, s51, 2048\n\t"
    "s_xor_b32 s66, s51, 65536\n\t"
    "s_add_u32 s63, s66, 4096\n\t"
    "s_add_u32 s69, s51, 4096\n\t"
    "s_xor_b32 s67, s51, 98304\n\t"
    "s_add_u32 s64, s67, 6144\n\t"
    "s_add_u32 s70, s51, 6144\n\t"
    "s_mov_b32 s32, s22\n\t"
    "s_and_b32 s33, s23, 0xffff\n\t"
    "s_mov_b32 s34, 0x1000000\n\t"
    "s_mov_b32 s35, 0x20000\n\t"
    "s_mov_b32 s84, 0xfb91aaa7\n\t"
    "s_mov_b32 s85, 0x8c73429b\n\t"
    "s_mov_b32 s86, 0x71939e51\n\t"
    "s_mov_b32 s87, 0xd0e5852f\n\t"
    "s_mov_b32 s88, 0x6965afad\n\t"
    "s_mov_b32 s89, 0x5fcf9f81\n\t"
    "s_mov_b32 s90, 0x82d7aed5\n\t"
    "s_mov_b32 s91, 0xe091cfe5\n\t"
    "s_lshl_b32 s31, s6, 12\n\t"
    "s_add_u32 s55, s31, 147456\n\t"
    "v_cmp_eq_u32 vcc, v7, v6\n\t"
    "v_and_b32 v11, 1, v8\n\t"
    "v_lshlrev_b32 v11, 4, v11\n\t"
    "v_mov_b32 v12, 0x3f80\n\t"
    "v_lshlrev_b32 v11, v11, v12\n\t"
    "v_cndmask_b32 v12, 0, v11, vcc\n\t"
    "v_cmp_gt_u32 s[60:61], 2, v8\n\t"
    "v_lshrrev_b32 v9, 1, v7\n\t"
    "v_and_b32 v10, 1, v6\n\t"
    "v_lshl_or_b32 v9, v10, 1, v9\n\t"
    "v_lshl_add_u32 v10, v6, 3, v7\n\t"
    "v_lshlrev_b32 v10, 7, v10\n\t"
    "v_lshl_add_u32 v10, v8, 3, v10\n\t"
    "v_lshl_add_u32 v1, v9, 5, v10\n\t"
    "v_xor_b32 v2, 32, v1\n\t"
    "v_xor_b32 v3, 64, v1\n\t"
    "v_xor_b32 v4, 0x60, v1\n\t"
    "v_bfe_u32 v9, v5, 1, 3\n\t"
    "v_xor_b32 v9, v9, v6\n\t"
    "v_lshlrev_b32 v9, 4, v9\n\t"
    "v_lshl_add_u32 v9, v5, 7, v9\n\t"
    "s_lshl_b32 s31, s6, 12\n\t"
    "v_add_u32 v9, s31, v9\n\t"
    "v_xor_b32 v10, 64, v9\n\t"
    "v_lshrrev_b32 v7, 1, v6\n\t"
    "v_xor_b32 v7, v7, v5\n\t"
    "v_lshlrev_b32 v7, 4, v7\n\t"
    "v_lshl_add_u32 v7, v5, 8, v7\n\t"
    "v_and_b32 v8, 1, v6\n\t"
    "v_lshl_add_u32 v7, v8, 3, v7\n\t"
    "s_lshl_b32 s31, s6, 13\n\t"
    "s_add_u32 s31, s31, 0x20000\n\t"
    "v_add_u32 v48, s31, v7\n\t"
    "v_lshlrev_b32 v5, 12, v5\n\t"
    "v_lshl_add_u32 v5, v8, 5, v5\n\t"
    "v_lshrrev_b32 v6, 1, v6\n\t"
    "v_lshl_add_u32 v5, v6, 4, v5\n\t"
    "v_add_u32 v6, s55, v14\n\t"
    "v_add_u32 v7, s55, v13\n\t"
    "s_mov_b32 s48, 0x75634120\n\t"
    "s_cmp_eq_u32 s27, 1\n\t"
    "s_cselect_b32 s48, 0x75643210, s48\n\t"
    "s_cmp_eq_u32 s27, 2\n\t"
    "s_cselect_b32 s48, 0x75e3c1a8, s48\n\t"
    "s_cmp_eq_u32 s27, 3\n\t"
    "s_cselect_b32 s48, 0x75ec3a18, s48\n\t"
    "v_xor_b32 v49, 32, v48\n\t"
    "v_xor_b32 v50, 64, v48\n\t"
    "v_xor_b32 v51, 96, v48\n\t"
    "v_xor_b32 v52, 128, v48\n\t"
    "v_xor_b32 v53, 160, v48\n\t"
    "v_xor_b32 v54, 192, v48\n\t"
    "v_xor_b32 v55, 224, v48\n\t"
    "v_mov_b64 v[64:65], 0\n\t"
    "v_mov_b64 v[66:67], 0\n\t"
    "s_nop 1\n\t"
    "v_mfma_f32_32x32x16_bf16 v[192:207], v[64:67], v[64:67], 0\n\t"
    ".Lhop4_%=:\n\t"
    "s_cbranch_execz .Lhr_%=\n\t"
    "v_mfma_f32_32x32x16_bf16 v[208:223], v[64:67], v[64:67], 0\n\t"
    "v_mfma_f32_32x32x16_bf16 v[224:239], v[64:67], v[64:67], 0\n\t"
    "v_mfma_f32_32x32x16_bf16 v[240:255], v[64:67], v[64:67], 0\n\t"
    "s_lshl_b32 s7, s27, 2\n\t"
    "s_add_u32 s7, s7, s6\n\t"
    "s_lshl_b32 s7, s7, 17\n\t"
    "s_lshl_b32 s19, s19, 8\n\t"
    "s_add_u32 s7, s7, s19\n\t"
    "s_lshl_b32 s19, s24, 11\n\t"
    "s_add_u32 s7, s7, s19\n\t"
    "s_lshl_b32 s19, s25, 6\n\t"
    "s_add_u32 s7, s7, s19\n\t"
    "s_add_u32 s40, s20, s7\n\t"
    "s_addc_u32 s41, s21, 0\n\t"
    "s_and_b32 s41, s41, 0xffff\n\t"
    "s_mov_b32 s43, 0x20000\n\t"
    "s_sub_u32 s42, 0x200000, s7\n\t"
    "s_mov_b32 s44, 7\n\t"
    "s_mov_b32 s45, 0\n\t"
    "s_mov_b32 s56, 0x3fb8aa3b\n\t"
    "s_mov_b32 s57, s56\n\t"
    "s_mov_b32 s92, 0x3db504f3\n\t"
    "s_mov_b32 s93, s92\n\t"
    "s_mov_b32 s94, 0xffff0000\n\t"
    "s_mov_b32 s58, 0x10000\n\t"
    "s_nop 0\n\t"
    "s_nop 0\n\t"
    "s_nop 0\n\t"
    "s_nop 0\n\t"
    "s_nop 0\n\t"
    "s_nop 0\n\t"
    "s_barrier\n\t"
    "ds_read_b128 v[64:67], v9 offset:0\n\t"
    "ds_read_b128 v[68:71], v10 offset:0\n\t"
    "ds_read_b128 v[72:75], v9 offset:2048\n\t"
    "ds_read_b128 v[76:79], v10 offset:2048\n\t"
    "ds_read_b64_tr_b16 v[16:17], v1 offset:16384\n\t"
    "ds_read_b64_tr_b16 v[18:19], v1 offset:16896\n\t"
    "ds_read_b64_tr_b16 v[20:21], v1 offset:20480\n\t"
    "ds_read_b64_tr_b16 v[22:23], v1 offset:20992\n\t"
    "ds_read_b64_tr_b16 v[24:25], v2 offset:16384\n\t"
    "ds_read_b64_tr_b16 v[26:27], v2 offset:16896\n\t"
    "ds_read_b64_tr_b16 v[28:29], v2 offset:20480\n\t"
    "ds_read_b64_tr_b16 v[30:31], v2 offset:20992\n\t"
    "ds_read_b64_tr_b16 v[32:33], v3 offset:16384\n\t"
    "ds_read_b64_tr_b16 v[34:35], v3 offset:16896\n\t"
    "ds_read_b64_tr_b16 v[36:37], v3 offset:20480\n\t"
    "ds_read_b64_tr_b16 v[38:39], v3 offset:20992\n\t"
    "s_waitcnt lgkmcnt(8)\n\t"
    "v_mfma_f32_16x16x32_bf16 v[128:131], v[16:19], v[64:67], 0\n\t"
    "ds_read_b64_tr_b16 v[40:41], v4 offset:16384\n\t"
    "v_mfma_f32_16x16x32_bf16 v[128:131], v[20:23], v[68:71], v[128:131]\n\t"
    "ds_read_b64_tr_b16 v[42:43], v4 offset:16896\n\t"
    "v_mfma_f32_16x16x32_bf16 v[160:163], v[16:19], v[72:75], 0\n\t"
    "ds_read_b64_tr_b16 v[44:45], v4 offset:20480\n\t"
    "v_mfma_f32_16x16x32_bf16 v[160:163], v[20:23], v[76:79], v[160:163]\n\t"
    "s_waitcnt lgkmcnt(7)\n\t"
    "ds_read_b64_tr_b16 v[46:47], v4 offset:20992\n\t"
    "v_mfma_f32_16x16x32_bf16 v[132:135], v[24:27], v[64:67], 0\n\t"
    "ds_read_b64_tr_b16 v[16:17], v1 offset:24576\n\t"
    "v_mfma_f32_16x16x32_bf16 v[132:135], v[28:31], v[68:71], v[132:135]\n\t"
    "ds_read_b64_tr_b16 v[18:19], v1 offset:25088\n\t"
    "v_mfma_f32_16x16x32_bf16 v[164:167], v[24:27], v[72:75], 0\n\t"
    "ds_read_b64_tr_b16 v[20:21], v1 offset:28672\n\t"
    "v_mfma_f32_16x16x32_bf16 v[164:167], v[28:31], v[76:79], v[164:167]\n\t"
    "s_waitcnt lgkmcnt(7)\n\t"
    "ds_read_b64_tr_b16 v[22:23], v1 offset:29184\n\t"
    "v_mfma_f32_16x16x32_bf16 v[136:139], v[32:35], v[64:67], 0\n\t"
    "ds_read_b64_tr_b16 v[24:25], v2 offset:24576\n\t"
    "v_mfma_f32_16x16x32_bf16 v[136:139], v[36:39], v[68:71], v[136:139]\n\t"
    "ds_read_b64_tr_b16 v[26:27], v2 offset:25088\n\t"
    "v_mfma_f32_16x16x32_bf16 v[168:171], v[32:35], v[72:75], 0\n\t"
    "ds_read_b64_tr_b16 v[28:29], v2 offset:28672\n\t"
    "v_mfma_f32_16x16x32_bf16 v[168:171], v[36:39], v[76:79], v[168:171]\n\t"
    "s_waitcnt lgkmcnt(7)\n\t"
    "ds_read_b64_tr_b16 v[30:31], v2 offset:29184\n\t"
    "v_mfma_f32_16x16x32_bf16 v[140:143], v[40:43], v[64:67], 0\n\t"
    "ds_read_b64_tr_b16 v[32:33], v3 offset:24576\n\t"
    "v_mfma_f32_16x16x32_bf16 v[140:143], v[44:47], v[68:71], v[140:143]\n\t"
    "ds_read_b64_tr_b16 v[34:35], v3 offset:25088\n\t"
    "v_mfma_f32_16x16x32_bf16 v[172:175], v[40:43], v[72:75], 0\n\t"
    "ds_read_b64_tr_b16 v[36:37], v3 offset:28672\n\t"
    "v_mfma_f32_16x16x32_bf16 v[172:175], v[44:47], v[76:79], v[172:175]\n\t"
    "s_waitcnt lgkmcnt(7)\n\t"
    "ds_read_b64_tr_b16 v[38:39], v3 offset:29184\n\t"
    "v_mfma_f32_16x16x32_bf16 v[144:147], v[16:19], v[64:67], 0\n\t"
    "ds_read_b64_tr_b16 v[40:41], v4 offset:24576\n\t"
    "v_mfma_f32_16x16x32_bf16 v[144:147], v[20:23], v[68:71], v[144:147]\n\t"
    "ds_read_b64_tr_b16 v[42:43], v4 offset:25088\n\t"
    "v_mfma_f32_16x16x32_bf16 v[176:179], v[16:19], v[72:75], 0\n\t"
    "ds_read_b64_tr_b16 v[44:45], v4 offset:28672\n\t"
    "v_mfma_f32_16x16x32_bf16 v[176:179], v[20:23], v[76:79], v[176:179]\n\t"
    "s_waitcnt lgkmcnt(7)\n\t"
    "ds_read_b64_tr_b16 v[46:47], v4 offset:29184\n\t"
    "v_mfma_f32_16x16x32_bf16 v[148:151], v[24:27], v[64:67], 0\n\t"
    "s_barrier\n\t"
    "ds_read_b64_tr_b16 v[16:17], v1 offset:49152\n\t"
    "v_mfma_f32_16x16x32_bf16 v[148:151], v[28:31], v[68:71], v[148:151]\n\t"
    "ds_read_b64_tr_b16 v[18:19], v1 offset:49664\n\t"
    "ds_read_b128 v[80:83], v9 offset:32768\n\t"
    "v_mfma_f32_16x16x32_bf16 v[180:183], v[24:27], v[72:75], 0\n\t"
    "ds_read_b64_tr_b16 v[20:21], v1 offset:53248\n\t"
    "ds_read_b128 v[84:87], v10 offset:32768\n\t"
    "v_mfma_f32_16x16x32_bf16 v[180:183], v[28:31], v[76:79], v[180:183]\n\t"
    "s_waitcnt lgkmcnt(9)\n\t"
    "ds_read_b64_tr_b16 v[22:23], v1 offset:53760\n\t"
    "v_mfma_f32_16x16x32_bf16 v[152:155], v[32:35], v[64:67], 0\n\t"
    "ds_read_b64_tr_b16 v[24:25], v2 offset:49152\n\t"
    "ds_read_b128 v[88:91], v9 offset:34816\n\t"
    "v_mfma_f32_16x16x32_bf16 v[152:155], v[36:39], v[68:71], v[152:155]\n\t"
    "ds_read_b64_tr_b16 v[26:27], v2 offset:49664\n\t"
    "ds_read_b128 v[92:95], v10 offset:34816\n\t"
    "v_mfma_f32_16x16x32_bf16 v[184:187], v[32:35], v[72:75], 0\n\t"
    "ds_read_b64_tr_b16 v[28:29], v2 offset:53248\n\t"
    "v_mfma_f32_16x16x32_bf16 v[184:187], v[36:39], v[76:79], v[184:187]\n\t"
    "s_waitcnt lgkmcnt(11)\n\t"
    "ds_read_b64_tr_b16 v[30:31], v2 offset:53760\n\t"
    "v_mfma_f32_16x16x32_bf16 v[156:159], v[40:43], v[64:67], 0\n\t"
    "ds_read_b64_tr_b16 v[32:33], v3 offset:49152\n\t"
    "v_mfma_f32_16x16x32_bf16 v[156:159], v[44:47], v[68:71], v[156:159]\n\t"
    "ds_read_b64_tr_b16 v[34:35], v3 offset:49664\n\t"
    "v_mfma_f32_16x16x32_bf16 v[188:191], v[40:43], v[72:75], 0\n\t"
    "ds_read_b64_tr_b16 v[36:37], v3 offset:53248\n\t"
    "v_mfma_f32_16x16x32_bf16 v[188:191], v[44:47], v[76:79], v[188:191]\n\t"
    "s_waitcnt lgkmcnt(5)\n\t"
    "ds_read_b64_tr_b16 v[38:39], v3 offset:53760\n\t"
    ".Lp1_%=:\n\t"
    "v_mfma_f32_16x16x32_bf16 v[128:131], v[16:19], v[80:83], v[128:131]\n\t"
    "ds_read_b64_tr_b16 v[40:41], v4 offset:49152\n\t"
    "v_xor_b32 v9, 0x10000, v9\n\t"
    "v_mfma_f32_16x16x32_bf16 v[128:131], v[20:23], v[84:87], v[128:131]\n\t"
    "ds_read_b64_tr_b16 v[42:43], v4 offset:49664\n\t"
    "v_xor_b32 v10, 0x10000, v10\n\t"
    "v_mfma_f32_16x16x32_bf16 v[160:163], v[16:19], v[88:91], v[160:163]\n\t"
    "ds_read_b64_tr_b16 v[44:45], v4 offset:53248\n\t"
    "v_mfma_f32_16x16x32_bf16 v[160:163], v[20:23], v[92:95], v[160:163]\n\t"
    "s_waitcnt lgkmcnt(7)\n\t"
    "ds_read_b64_tr_b16 v[46:47], v4 offset:53760\n\t"
    "v_mfma_f32_16x16x32_bf16 v[132:135], v[24:27], v[80:83], v[132:135]\n\t"
    "ds_read_b64_tr_b16 v[16:17], v1 offset:57344\n\t"
    "v_mfma_f32_16x16x32_bf16 v[132:135], v[28:31], v[84:87], v[132:135]\n\t"
    "ds_read_b64_tr_b16 v[18:19], v1 offset:57856\n\t"
    "v_mfma_f32_16x16x32_bf16 v[164:167], v[24:27], v[88:91], v[164:167]\n\t"
    "ds_read_b64_tr_b16 v[20:21], v1 offset:61440\n\t"
    "v_mfma_f32_16x16x32_bf16 v[164:167], v[28:31], v[92:95], v[164:167]\n\t"
    "s_waitcnt lgkmcnt(7)\n\t"
    "ds_read_b64_tr_b16 v[22:23], v1 offset:61952\n\t"
    "v_mfma_f32_16x16x32_bf16 v[136:139], v[32:35], v[80:83], v[136:139]\n\t"
    "ds_read_b64_tr_b16 v[24:25], v2 offset:57344\n\t"
    "v_mfma_f32_16x16x32_bf16 v[136:139], v[36:39], v[84:87], v[136:139]\n\t"
    "ds_read_b64_tr_b16 v[26:27], v2 offset:57856\n\t"
    "v_mfma_f32_16x16x32_bf16 v[168:171], v[32:35], v[88:91], v[168:171]\n\t"
    "ds_read_b64_tr_b16 v[28:29], v2 offset:61440\n\t"
    "v_mfma_f32_16x16x32_bf16 v[168:171], v[36:39], v[92:95], v[168:171]\n\t"
    "s_waitcnt lgkmcnt(7)\n\t"
    "ds_read_b64_tr_b16 v[30:31], v2 offset:61952\n\t"
    "v_mfma_f32_16x16x32_bf16 v[140:143], v[40:43], v[80:83], v[140:143]\n\t"
    "ds_read_b64_tr_b16 v[32:33], v3 offset:57344\n\t"
    "v_mfma_f32_16x16x32_bf16 v[140:143], v[44:47], v[84:87], v[140:143]\n\t"
    "ds_read_b64_tr_b16 v[34:35], v3 offset:57856\n\t"
    "v_mfma_f32_16x16x32_bf16 v[172:175], v[40:43], v[88:91], v[172:175]\n\t"
    "ds_read_b64_tr_b16 v[36:37], v3 offset:61440\n\t"
    "v_xor_b32 v1, 0x10000, v1\n\t"
    "v_mfma_f32_16x16x32_bf16 v[172:175], v[44:47], v[92:95], v[172:175]\n\t"
    "s_waitcnt lgkmcnt(7)\n\t"
    "ds_read_b64_tr_b16 v[38:39], v3 offset:61952\n\t"
    "v_mfma_f32_16x16x32_bf16 v[144:147], v[16:19], v[80:83], v[144:147]\n\t"
    "ds_read_b64_tr_b16 v[40:41], v4 offset:57344\n\t"
    "v_mfma_f32_16x16x32_bf16 v[144:147], v[20:23], v[84:87], v[144:147]\n\t"
    "ds_read_b64_tr_b16 v[42:43], v4 offset:57856\n\t"
    "v_mfma_f32_16x16x32_bf16 v[176:179], v[16:19], v[88:91], v[176:179]\n\t"
    "ds_read_b64_tr_b16 v[44:45], v4 offset:61440\n\t"
    "v_xor_b32 v2, 0x10000, v2\n\t"
    "v_mfma_f32_16x16x32_bf16 v[176:179], v[20:23], v[92:95], v[176:179]\n\t"
    "s_waitcnt lgkmcnt(7)\n\t"
    "ds_read_b64_tr_b16 v[46:47], v4 offset:61952\n\t"
    "v_mfma_f32_16x16x32_bf16 v[148:151], v[24:27], v[80:83], v[148:151]\n\t"
    "s_barrier\n\t"
    "ds_read_b64_tr_b16 v[16:17], v1 offset:16384\n\t"
    "v_mfma_f32_16x16x32_bf16 v[148:151], v[28:31], v[84:87], v[148:151]\n\t"
    "ds_read_b64_tr_b16 v[18:19], v1 offset:16896\n\t"
    "ds_read_b128 v[64:67], v9 offset:0\n\t"
    "v_mfma_f32_16x16x32_bf16 v[180:183], v[24:27], v[88:91], v[180:183]\n\t"
    "ds_read_b64_tr_b16 v[20:21], v1 offset:20480\n\t"
    "ds_read_b128 v[68:71], v10 offset:0\n\t"
    "v_mfma_f32_16x16x32_bf16 v[180:183], v[28:31], v[92:95], v[180:183]\n\t"
    "s_waitcnt lgkmcnt(9)\n\t"
    "ds_read_b64_tr_b16 v[22:23], v1 offset:20992\n\t"
    "v_mfma_f32_16x16x32_bf16 v[152:155], v[32:35], v[80:83], v[152:155]\n\t"
    "ds_read_b64_tr_b16 v[24:25], v2 offset:16384\n\t"
    "ds_read_b128 v[72:75], v9 offset:2048\n\t"
    "v_mfma_f32_16x16x32_bf16 v[152:155], v[36:39], v[84:87], v[152:155]\n\t"
    "ds_read_b64_tr_b16 v[26:27], v2 offset:16896\n\t"
    "ds_read_b128 v[76:79], v10 offset:2048\n\t"
    "v_mfma_f32_16x16x32_bf16 v[184:187], v[32:35], v[88:91], v[184:187]\n\t"
    "ds_read_b64_tr_b16 v[28:29], v2 offset:20480\n\t"
    "v_xor_b32 v3, 0x10000, v3\n\t"
    "v_mfma_f32_16x16x32_bf16 v[184:187], v[36:39], v[92:95], v[184:187]\n\t"
    "s_waitcnt lgkmcnt(11)\n\t"
    "ds_read_b64_tr_b16 v[30:31], v2 offset:20992\n\t"
    "v_mfma_f32_16x16x32_bf16 v[156:159], v[40:43], v[80:83], v[156:159]\n\t"
    "ds_read_b64_tr_b16 v[32:33], v3 offset:16384\n\t"
    "v_xor_b32 v4, 0x10000, v4\n\t"
    "v_mfma_f32_16x16x32_bf16 v[156:159], v[44:47], v[84:87], v[156:159]\n\t"
    "ds_read_b64_tr_b16 v[34:35], v3 offset:16896\n\t"
    "v_mfma_f32_16x16x32_bf16 v[188:191], v[40:43], v[88:91], v[188:191]\n\t"
    "ds_read_b64_tr_b16 v[36:37], v3 offset:20480\n\t"
    "v_mfma_f32_16x16x32_bf16 v[188:191], v[44:47], v[92:95], v[188:191]\n\t"
    "s_waitcnt lgkmcnt(5)\n\t"
    "ds_read_b64_tr_b16 v[38:39], v3 offset:20992\n\t"
    "v_mfma_f32_16x16x32_bf16 v[128:131], v[16:19], v[64:67], v[128:131]\n\t"
    "ds_read_b64_tr_b16 v[40:41], v4 offset:16384\n\t"
    "v_mfma_f32_16x16x32_bf16 v[128:131], v[20:23], v[68:71], v[128:131]\n\t"
    "ds_read_b64_tr_b16 v[42:43], v4 offset:16896\n\t"
    "v_mfma_f32_16x16x32_bf16 v[160:163], v[16:19], v[72:75], v[160:163]\n\t"
    "ds_read_b64_tr_b16 v[44:45], v4 offset:20480\n\t"
    "v_mfma_f32_16x16x32_bf16 v[160:163], v[20:23], v[76:79], v[160:163]\n\t"
    "s_waitcnt lgkmcnt(7)\n\t"
    "ds_read_b64_tr_b16 v[46:47], v4 offset:20992\n\t"
    "v_mfma_f32_16x16x32_bf16 v[132:135], v[24:27], v[64:67], v[132:135]\n\t"
    "ds_read_b64_tr_b16 v[16:17], v1 offset:24576\n\t"
    "v_mfma_f32_16x16x32_bf16 v[132:135], v[28:31], v[68:71], v[132:135]\n\t"
    "ds_read_b64_tr_b16 v[18:19], v1 offset:25088\n\t"
    "v_mfma_f32_16x16x32_bf16 v[164:167], v[24:27], v[72:75], v[164:167]\n\t"
    "ds_read_b64_tr_b16 v[20:21], v1 offset:28672\n\t"
    "v_mfma_f32_16x16x32_bf16 v[164:167], v[28:31], v[76:79], v[164:167]\n\t"
    "s_waitcnt lgkmcnt(7)\n\t"
    "ds_read_b64_tr_b16 v[22:23], v1 offset:29184\n\t"
    "v_mfma_f32_16x16x32_bf16 v[136:139], v[32:35], v[64:67], v[136:139]\n\t"
    "ds_read_b64_tr_b16 v[24:25], v2 offset:24576\n\t"
    "v_mfma_f32_16x16x32_bf16 v[136:139], v[36:39], v[68:71], v[136:139]\n\t"
    "ds_read_b64_tr_b16 v[26:27], v2 offset:25088\n\t"
    "v_mfma_f32_16x16x32_bf16 v[168:171], v[32:35], v[72:75], v[168:171]\n\t"
    "ds_read_b64_tr_b16 v[28:29], v2 offset:28672\n\t"
    "v_mfma_f32_16x16x32_bf16 v[168:171], v[36:39], v[76:79], v[168:171]\n\t"
    "s_waitcnt lgkmcnt(7)\n\t"
    "ds_read_b64_tr_b16 v[30:31], v2 offset:29184\n\t"
    "v_mfma_f32_16x16x32_bf16 v[140:143], v[40:43], v[64:67], v[140:143]\n\t"
    "ds_read_b64_tr_b16 v[32:33], v3 offset:24576\n\t"
    "v_mfma_f32_16x16x32_bf16 v[140:143], v[44:47], v[68:71], v[140:143]\n\t"
    "ds_read_b64_tr_b16 v[34:35], v3 offset:25088\n\t"
    "v_mfma_f32_16x16x32_bf16 v[172:175], v[40:43], v[72:75], v[172:175]\n\t"
    "ds_read_b64_tr_b16 v[36:37], v3 offset:28672\n\t"
    "v_mfma_f32_16x16x32_bf16 v[172:175], v[44:47], v[76:79], v[172:175]\n\t"
    "s_waitcnt lgkmcnt(7)\n\t"
    "ds_read_b64_tr_b16 v[38:39], v3 offset:29184\n\t"
    "v_mfma_f32_16x16x32_bf16 v[144:147], v[16:19], v[64:67], v[144:147]\n\t"
    "ds_read_b64_tr_b16 v[40:41], v4 offset:24576\n\t"
    "v_mfma_f32_16x16x32_bf16 v[144:147], v[20:23], v[68:71], v[144:147]\n\t"
    "ds_read_b64_tr_b16 v[42:43], v4 offset:25088\n\t"
    "v_mfma_f32_16x16x32_bf16 v[176:179], v[16:19], v[72:75], v[176:179]\n\t"
    "ds_read_b64_tr_b16 v[44:45], v4 offset:28672\n\t"
    "v_mfma_f32_16x16x32_bf16 v[176:179], v[20:23], v[76:79], v[176:179]\n\t"
    "s_waitcnt lgkmcnt(7)\n\t"
    "ds_read_b64_tr_b16 v[46:47], v4 offset:29184\n\t"
    "v_mfma_f32_16x16x32_bf16 v[148:151], v[24:27], v[64:67], v[148:151]\n\t"
    "s_barrier\n\t"
    "ds_read_b64_tr_b16 v[16:17], v1 offset:49152\n\t"
    "v_mfma_f32_16x16x32_bf16 v[148:151], v[28:31], v[68:71], v[148:151]\n\t"
    "ds_read_b64_tr_b16 v[18:19], v1 offset:49664\n\t"
    "ds_read_b128 v[80:83], v9 offset:32768\n\t"
    "v_mfma_f32_16x16x32_bf16 v[180:183], v[24:27], v[72:75], v[180:183]\n\t"
    "ds_read_b64_tr_b16 v[20:21], v1 offset:53248\n\t"
    "ds_read_b128 v[84:87], v10 offset:32768\n\t"
    "v_mfma_f32_16x16x32_bf16 v[180:183], v[28:31], v[76:79], v[180:183]\n\t"
    "s_waitcnt lgkmcnt(9)\n\t"
    "ds_read_b64_tr_b16 v[22:23], v1 offset:53760\n\t"
    "v_mfma_f32_16x16x32_bf16 v[152:155], v[32:35], v[64:67], v[152:155]\n\t"
    "ds_read_b64_tr_b16 v[24:25], v2 offset:49152\n\t"
    "ds_read_b128 v[88:91], v9 offset:34816\n\t"
    "v_mfma_f32_16x16x32_bf16 v[152:155], v[36:39], v[68:71], v[152:155]\n\t"
    "ds_read_b64_tr_b16 v[26:27], v2 offset:49664\n\t"
    "ds_read_b128 v[92:95], v10 offset:34816\n\t"
    "v_mfma_f32_16x16x32_bf16 v[184:187], v[32:35], v[72:75], v[184:187]\n\t"
    "ds_read_b64_tr_b16 v[28:29], v2 offset:53248\n\t"
    "v_mfma_f32_16x16x32_bf16 v[184:187], v[36:39], v[76:79], v[184:187]\n\t"
    "s_waitcnt lgkmcnt(11)\n\t"
    "ds_read_b64_tr_b16 v[30:31], v2 offset:53760\n\t"
    "v_mfma_f32_16x16x32_bf16 v[156:159], v[40:43], v[64:67], v[156:159]\n\t"
    "ds_read_b64_tr_b16 v[32:33], v3 offset:49152\n\t"
    "v_mfma_f32_16x16x32_bf16 v[156:159], v[44:47], v[68:71], v[156:159]\n\t"
    "s_sub_u32 s44, s44, 1\n\t"
    "ds_read_b64_tr_b16 v[34:35], v3 offset:49664\n\t"
    "v_mfma_f32_16x16x32_bf16 v[188:191], v[40:43], v[72:75], v[188:191]\n\t"
    "s_cmp_lg_u32 s44, 0\n\t"
    "ds_read_b64_tr_b16 v[36:37], v3 offset:53248\n\t"
    "v_mfma_f32_16x16x32_bf16 v[188:191], v[44:47], v[76:79], v[188:191]\n\t"
    "s_waitcnt lgkmcnt(5)\n\t"
    "ds_read_b64_tr_b16 v[38:39], v3 offset:53760\n\t"
    "s_cbranch_scc1 .Lp1_%=\n\t"
    ".Lhop5_%=:\n\t"
    "s_cbranch_execz .Lhr_%=\n\t"
    "v_mfma_f32_16x16x32_bf16 v[128:131], v[16:19], v[80:83], v[128:131]\n\t"
    "ds_read_b64_tr_b16 v[40:41], v4 offset:49152\n\t"
    "v_xor_b32 v9, 0x10000, v9\n\t"
    "v_mfma_f32_16x16x32_bf16 v[128:131], v[20:23], v[84:87], v[128:131]\n\t"
    "ds_read_b64_tr_b16 v[42:43], v4 offset:49664\n\t"
    "v_xor_b32 v10, 0x10000, v10\n\t"
    "v_mfma_f32_16x16x32_bf16 v[160:163], v[16:19], v[88:91], v[160:163]\n\t"
    "ds_read_b64_tr_b16 v[44:45], v4 offset:53248\n\t"
    "s_nop 0\n\t"
    "v_mfma_f32_16x16x32_bf16 v[160:163], v[20:23], v[92:95], v[160:163]\n\t"
    "s_waitcnt lgkmcnt(7)\n\t"
    "ds_read_b64_tr_b16 v[46:47], v4 offset:53760\n\t"
    "s_nop 0\n\t"
    "v_mfma_f32_16x16x32_bf16 v[132:135], v[24:27], v[80:83], v[132:135]\n\t"
    "ds_read_b64_tr_b16 v[16:17], v1 offset:57344\n\t"
    "v_cvt_pk_bf16_f32 v128, v128, v129\n\t"
    "v_mfma_f32_16x16x32_bf16 v[132:135], v[28:31], v[84:87], v[132:135]\n\t"
    "ds_read_b64_tr_b16 v[18:19], v1 offset:57856\n\t"
    "v_cvt_pk_bf16_f32 v129, v130, v131\n\t"
    "v_mfma_f32_16x16x32_bf16 v[164:167], v[24:27], v[88:91], v[164:167]\n\t"
    "ds_read_b64_tr_b16 v[20:21], v1 offset:61440\n\t"
    "v_cvt_pk_bf16_f32 v160, v160, v161\n\t"
    "v_mfma_f32_16x16x32_bf16 v[164:167], v[28:31], v[92:95], v[164:167]\n\t"
    "s_waitcnt lgkmcnt(7)\n\t"
    "ds_read_b64_tr_b16 v[22:23], v1 offset:61952\n\t"
    "v_xor_b32 v1, 0x10000, v1\n\t"
    "v_mfma_f32_16x16x32_bf16 v[136:139], v[32:35], v[80:83], v[136:139]\n\t"
    "ds_read_b64_tr_b16 v[24:25], v2 offset:57344\n\t"
    "v_cvt_pk_bf16_f32 v130, v132, v133\n\t"
    "v_mfma_f32_16x16x32_bf16 v[136:139], v[36:39], v[84:87], v[136:139]\n\t"
    "ds_read_b64_tr_b16 v[26:27], v2 offset:57856\n\t"
    "v_cvt_pk_bf16_f32 v131, v134, v135\n\t"
    "v_mfma_f32_16x16x32_bf16 v[168:171], v[32:35], v[88:91], v[168:171]\n\t"
    "ds_read_b64_tr_b16 v[28:29], v2 offset:61440\n\t"
    "v_cvt_pk_bf16_f32 v161, v162, v163\n\t"
    "v_mfma_f32_16x16x32_bf16 v[168:171], v[36:39], v[92:95], v[168:171]\n\t"
    "s_waitcnt lgkmcnt(7)\n\t"
    "ds_read_b64_tr_b16 v[30:31], v2 offset:61952\n\t"
    "v_xor_b32 v2, 0x10000, v2\n\t"
    "v_mfma_f32_16x16x32_bf16 v[140:143], v[40:43], v[80:83], v[140:143]\n\t"
    "ds_read_b64_tr_b16 v[32:33], v3 offset:57344\n\t"
    "v_cvt_pk_bf16_f32 v162, v164, v165\n\t"
    "v_mfma_f32_16x16x32_bf16 v[140:143], v[44:47], v[84:87], v[140:143]\n\t"
    "ds_read_b64_tr_b16 v[34:35], v3 offset:57856\n\t"
    "v_cvt_pk_bf16_f32 v163, v166, v167\n\t"
    "v_mfma_f32_16x16x32_bf16 v[172:175], v[40:43], v[88:91], v[172:175]\n\t"
    "ds_read_b64_tr_b16 v[36:37], v3 offset:61440\n\t"
    "v_cvt_pk_bf16_f32 v136, v136, v137\n\t"
    "v_mfma_f32_16x16x32_bf16 v[172:175], v[44:47], v[92:95], v[172:175]\n\t"
    "s_waitcnt lgkmcnt(7)\n\t"
    "ds_read_b64_tr_b16 v[38:39], v3 offset:61952\n\t"
    "v_xor_b32 v3, 0x10000, v3\n\t"
    "v_mfma_f32_16x16x32_bf16 v[144:147], v[16:19], v[80:83], v[144:147]\n\t"
    "ds_read_b64_tr_b16 v[40:41], v4 offset:57344\n\t"
    "v_cvt_pk_bf16_f32 v137, v138, v139\n\t"
    "v_mfma_f32_16x16x32_bf16 v[144:147], v[20:23], v[84:87], v[144:147]\n\t"
    "ds_read_b64_tr_b16 v[42:43], v4 offset:57856\n\t"
    "v_cvt_pk_bf16_f32 v138, v140, v141\n\t"
    "v_mfma_f32_16x16x32_bf16 v[176:179], v[16:19], v[88:91], v[176:179]\n\t"
    "ds_read_b64_tr_b16 v[44:45], v4 offset:61440\n\t"
    "v_cvt_pk_bf16_f32 v139, v142, v143\n\t"
    "v_mfma_f32_16x16x32_bf16 v[176:179], v[20:23], v[92:95], v[176:179]\n\t"
    "s_waitcnt lgkmcnt(7)\n\t"
    "ds_read_b64_tr_b16 v[46:47], v4 offset:61952\n\t"
    "v_xor_b32 v4, 0x10000, v4\n\t"
    "v_mfma_f32_16x16x32_bf16 v[148:151], v[24:27], v[80:83], v[148:151]\n\t"
    "ds_read_b64 v[96:97], v48 offset:0\n\t"
    "v_cvt_pk_bf16_f32 v168, v168, v169\n\t"
    "v_mfma_f32_16x16x32_bf16 v[148:151], v[28:31], v[84:87], v[148:151]\n\t"
    "ds_read_b64 v[98:99], v49 offset:0\n\t"
    "v_cvt_pk_bf16_f32 v169, v170, v171\n\t"
    "v_mfma_f32_16x16x32_bf16 v[180:183], v[24:27], v[88:91], v[180:183]\n\t"
    "ds_read_b64 v[112:113], v48 offset:4096\n\t"
    "v_cvt_pk_bf16_f32 v170, v172, v173\n\t"
    "v_mfma_f32_16x16x32_bf16 v[180:183], v[28:31], v[92:95], v[180:183]\n\t"
    "s_waitcnt lgkmcnt(7)\n\t"
    "ds_read_b64 v[114:115], v49 offset:4096\n\t"
    "v_cvt_pk_bf16_f32 v171, v174, v175\n\t"
    "v_mfma_f32_16x16x32_bf16 v[152:155], v[32:35], v[80:83], v[152:155]\n\t"
    "ds_read_b64 v[100:101], v50 offset:0\n\t"
    "v_cvt_pk_bf16_f32 v144, v144, v145\n\t"
    "v_mfma_f32_16x16x32_bf16 v[152:155], v[36:39], v[84:87], v[152:155]\n\t"
    "ds_read_b64 v[102:103], v51 offset:0\n\t"
    "v_cvt_pk_bf16_f32 v145, v146, v147\n\t"
    "v_mfma_f32_16x16x32_bf16 v[184:187], v[32:35], v[88:91], v[184:187]\n\t"
    "ds_read_b64 v[116:117], v50 offset:4096\n\t"
    "v_cvt_pk_bf16_f32 v146, v148, v149\n\t"
    "v_mfma_f32_16x16x32_bf16 v[184:187], v[36:39], v[92:95], v[184:187]\n\t"
    "s_waitcnt lgkmcnt(7)\n\t"
    "ds_read_b64 v[118:119], v51 offset:4096\n\t"
    "v_cvt_pk_bf16_f32 v147, v150, v151\n\t"
    "v_mfma_f32_16x16x32_bf16 v[156:159], v[40:43], v[80:83], v[156:159]\n\t"
    "ds_read_b64 v[104:105], v52 offset:0\n\t"
    "v_cvt_pk_bf16_f32 v176, v176, v177\n\t"
    "v_mfma_f32_16x16x32_bf16 v[156:159], v[44:47], v[84:87], v[156:159]\n\t"
    "ds_read_b64 v[106:107], v53 offset:0\n\t"
    "v_cvt_pk_bf16_f32 v177, v178, v179\n\t"
    "v_mfma_f32_16x16x32_bf16 v[188:191], v[40:43], v[88:91], v[188:191]\n\t"
    "ds_read_b64 v[120:121], v52 offset:4096\n\t"
    "v_cvt_pk_bf16_f32 v178, v180, v181\n\t"
    "v_mfma_f32_16x16x32_bf16 v[188:191], v[44:47], v[92:95], v[188:191]\n\t"
    "s_waitcnt lgkmcnt(9)\n\t"
    "ds_read_b64 v[122:123], v53 offset:4096\n\t"
    "v_cvt_pk_bf16_f32 v179, v182, v183\n\t"
    "v_mfma_f32_16x16x32_bf16 v[132:135], v[96:99], v[128:131], 0\n\t"
    "s_waitcnt lgkmcnt(8)\n\t"
    "ds_read_b64 v[108:109], v54 offset:0\n\t"
    "v_cvt_pk_bf16_f32 v152, v152, v153\n\t"
    "v_mfma_f32_16x16x32_bf16 v[140:143], v[112:115], v[128:131], 0\n\t"
    "s_waitcnt lgkmcnt(7)\n\t"
    "ds_read_b64 v[110:111], v55 offset:0\n\t"
    "v_cvt_pk_bf16_f32 v153, v154, v155\n\t"
    "v_mfma_f32_16x16x32_bf16 v[132:135], v[100:103], v[136:139], v[132:135]\n\t"
    "s_waitcnt lgkmcnt(6)\n\t"
    "ds_read_b64 v[124:125], v54 offset:4096\n\t"
    "v_cvt_pk_bf16_f32 v154, v156, v157\n\t"
    "v_mfma_f32_16x16x32_bf16 v[140:143], v[116:119], v[136:139], v[140:143]\n\t"
    "s_waitcnt lgkmcnt(5)\n\t"
    "ds_read_b64 v[126:127], v55 offset:4096\n\t"
    "v_cvt_pk_bf16_f32 v155, v158, v159\n\t"
    "v_mfma_f32_16x16x32_bf16 v[132:135], v[104:107], v[144:147], v[132:135]\n\t"
    "s_waitcnt lgkmcnt(4)\n\t"
    "v_cvt_pk_bf16_f32 v184, v184, v185\n\t"
    "v_cvt_pk_bf16_f32 v185, v186, v187\n\t"
    "v_mfma_f32_16x16x32_bf16 v[140:143], v[120:123], v[144:147], v[140:143]\n\t"
    "v_cvt_pk_bf16_f32 v186, v188, v189\n\t"
    "v_cvt_pk_bf16_f32 v187, v190, v191\n\t"
    "v_mfma_f32_16x16x32_bf16 v[164:167], v[96:99], v[160:163], 0\n\t"
    "s_waitcnt lgkmcnt(2)\n\t"
    ".Lhop6_%=:\n\t"
    "s_cbranch_execz .Lhr_%=\n\t"
    "v_mfma_f32_16x16x32_bf16 v[132:135], v[108:111], v[152:155], v[132:135]\n\t"
    "s_nop 1\n\t"
    "v_mfma_f32_16x16x32_bf16 v[172:175], v[112:115], v[160:163], 0\n\t"
    "s_waitcnt lgkmcnt(0)\n\t"
    "s_nop 1\n\t"
    "v_mfma_f32_16x16x32_bf16 v[140:143], v[124:127], v[152:155], v[140:143]\n\t"
    "s_nop 1\n\t"
    "v_mfma_f32_16x16x32_bf16 v[164:167], v[100:103], v[168:171], v[164:167]\n\t"
    "v_cvt_pk_bf16_f32 v148, v132, v133\n\t"
    "v_cvt_pk_bf16_f32 v149, v134, v135\n\t"
    "v_mfma_f32_16x16x32_bf16 v[172:175], v[116:119], v[168:171], v[172:175]\n\t"
    "v_lshlrev_b32 v132, 16, v148\n\t"
    "v_and_b32 v133, s94, v148\n\t"
    "v_mfma_f32_16x16x32_bf16 v[164:167], v[104:107], v[176:179], v[164:167]\n\t"
    "v_cvt_pk_bf16_f32 v150, v140, v141\n\t"
    "v_cvt_pk_bf16_f32 v151, v142, v143\n\t"
    "v_mfma_f32_16x16x32_bf16 v[172:175], v[120:123], v[176:179], v[172:175]\n\t"
    "v_lshlrev_b32 v134, 16, v149\n\t"
    "v_and_b32 v135, s94, v149\n\t"
    "v_mfma_f32_16x16x32_bf16 v[164:167], v[108:111], v[184:187], v[164:167]\n\t"
    "v_lshlrev_b32 v140, 16, v150\n\t"
    "v_and_b32 v141, s94, v150\n\t"
    "v_mfma_f32_16x16x32_bf16 v[172:175], v[124:127], v[184:187], v[172:175]\n\t"
    "v_lshlrev_b32 v142, 16, v151\n\t"
    "v_and_b32 v143, s94, v151\n\t"
    "s_waitcnt vmcnt(0)\n\t"
    "v_add_u32 v63, 1, v63\n\t"
    "s_nop 0\n\t"
    "v_readlane_b32 s46, v63, 0\n\t"
    "v_readlane_b32 s47, v63, 1\n\t"
    "s_nop 0\n\t"
    "v_mov_b32 v11, s46\n\t"
    "v_pk_mul_f32 v[132:133], v[132:133], s[92:93]\n\t"
    "v_pk_mul_f32 v[134:135], v[134:135], s[92:93]\n\t"
    "v_pk_mul_f32 v[140:141], v[140:141], s[92:93]\n\t"
    "v_pk_mul_f32 v[142:143], v[142:143], s[92:93]\n\t"
    "v_cvt_pk_bf16_f32 v148, v132, v133\n\t"
    "v_cvt_pk_bf16_f32 v149, v134, v135\n\t"
    "v_cvt_pk_bf16_f32 v150, v140, v141\n\t"
    "v_cvt_pk_bf16_f32 v151, v142, v143\n\t"
    "v_lshlrev_b32 v132, 16, v148\n\t"
    "v_and_b32 v133, s94, v148\n\t"
    "v_lshlrev_b32 v134, 16, v149\n\t"
    "v_and_b32 v135, s94, v149\n\t"
    "v_lshlrev_b32 v140, 16, v150\n\t"
    "v_and_b32 v141, s94, v150\n\t"
    "v_lshlrev_b32 v142, 16, v151\n\t"
    "v_and_b32 v143, s94, v151\n\t"
    "v_cvt_pk_bf16_f32 v180, v164, v165\n\t"
    "v_cvt_pk_bf16_f32 v181, v166, v167\n\t"
    "v_cvt_pk_bf16_f32 v182, v172, v173\n\t"
    "v_cvt_pk_bf16_f32 v183, v174, v175\n\t"
    "v_lshlrev_b32 v164, 16, v180\n\t"
    "v_and_b32 v165, s94, v180\n\t"
    "v_lshlrev_b32 v166, 16, v181\n\t"
    "v_and_b32 v167, s94, v181\n\t"
    "v_lshlrev_b32 v172, 16, v182\n\t"
    "v_and_b32 v173, s94, v182\n\t"
    "v_lshlrev_b32 v174, 16, v183\n\t"
    "v_and_b32 v175, s94, v183\n\t"
    "v_pk_mul_f32 v[164:165], v[164:165], s[92:93]\n\t"
    "v_pk_mul_f32 v[166:167], v[166:167], s[92:93]\n\t"
    "v_pk_mul_f32 v[172:173], v[172:173], s[92:93]\n\t"
    "v_pk_mul_f32 v[174:175], v[174:175], s[92:93]\n\t"
    "v_cvt_pk_bf16_f32 v180, v164, v165\n\t"
    "v_cvt_pk_bf16_f32 v181, v166, v167\n\t"
    "v_cvt_pk_bf16_f32 v182, v172, v173\n\t"
    "v_cvt_pk_bf16_f32 v183, v174, v175\n\t"
    "v_lshlrev_b32 v164, 16, v180\n\t"
    "v_and_b32 v165, s94, v180\n\t"
    "v_lshlrev_b32 v166, 16, v181\n\t"
    "v_and_b32 v167, s94, v181\n\t"
    "v_lshlrev_b32 v172, 16, v182\n\t"
    "v_and_b32 v173, s94, v182\n\t"
    "v_lshlrev_b32 v174, 16, v183\n\t"
    "v_and_b32 v175, s94, v183\n\t"
    "v_max3_f32 v96, v132, v133, v134\n\t"
    "v_max3_f32 v98, v164, v165, v166\n\t"
    "v_max3_f32 v100, v135, v140, v141\n\t"
    "v_max3_f32 v102, v167, v172, v173\n\t"
    "v_max3_f32 v96, v96, v142, v143\n\t"
    "v_max3_f32 v98, v98, v174, v175\n\t"
    "v_max_f32 v96, v96, v100\n\t"
    "v_max_f32 v98, v98, v102\n\t"
    "v_mov_b32 v100, v96\n\t"
    "v_mov_b32 v102, v98\n\t"
    "s_nop 0\n\t"
    "v_permlane16_swap_b32 v96, v100\n\t"
    "v_permlane16_swap_b32 v98, v102\n\t"
    "v_max_f32 v96, v96, v100\n\t"
    "v_max_f32 v98, v98, v102\n\t"
    "v_mov_b32 v100, v96\n\t"
    "v_mov_b32 v102, v98\n\t"
    "s_nop 0\n\t"
    "v_permlane32_swap_b32 v96, v100\n\t"
    "v_permlane32_swap_b32 v98, v102\n\t"
    "v_max_f32 v96, v96, v100\n\t"
    "v_max_f32 v98, v98, v102\n\t"
    "v_mul_f32 v96, s57, v96\n\t"
    "v_mul_f32 v98, s57, v98\n\t"
    "v_pk_fma_f32 v[132:133], v[132:133], s[56:57], v[96:97] op_sel_hi:[1,1,0] neg_lo:[0,0,1] neg_hi:[0,0,1]\n\t"
    "v_pk_fma_f32 v[164:165], v[164:165], s[56:57], v[98:99] op_sel_hi:[1,1,0] neg_lo:[0,0,1] neg_hi:[0,0,1]\n\t"
    "v_pk_fma_f32 v[134:135], v[134:135], s[56:57], v[96:97] op_sel_hi:[1,1,0] neg_lo:[0,0,1] neg_hi:[0,0,1]\n\t"
    "v_pk_fma_f32 v[166:167], v[166:167], s[56:57], v[98:99] op_sel_hi:[1,1,0] neg_lo:[0,0,1] neg_hi:[0,0,1]\n\t"
    "v_pk_fma_f32 v[140:141], v[140:141], s[56:57], v[96:97] op_sel_hi:[1,1,0] neg_lo:[0,0,1] neg_hi:[0,0,1]\n\t"
    "v_pk_fma_f32 v[172:173], v[172:173], s[56:57], v[98:99] op_sel_hi:[1,1,0] neg_lo:[0,0,1] neg_hi:[0,0,1]\n\t"
    "v_pk_fma_f32 v[142:143], v[142:143], s[56:57], v[96:97] op_sel_hi:[1,1,0] neg_lo:[0,0,1] neg_hi:[0,0,1]\n\t"
    "v_pk_fma_f32 v[174:175], v[174:175], s[56:57], v[98:99] op_sel_hi:[1,1,0] neg_lo:[0,0,1] neg_hi:[0,0,1]\n\t"
    "v_exp_f32 v132, v132\n\t"
    "v_exp_f32 v164, v164\n\t"
    "v_exp_f32 v133, v133\n\t"
    "v_exp_f32 v165, v165\n\t"
    "v_exp_f32 v134, v134\n\t"
    "v_exp_f32 v166, v166\n\t"
    "v_exp_f32 v135, v135\n\t"
    "v_exp_f32 v167, v167\n\t"
    "v_exp_f32 v140, v140\n\t"
    "v_exp_f32 v172, v172\n\t"
    "v_exp_f32 v141, v141\n\t"
    "v_exp_f32 v173, v173\n\t"
    "v_exp_f32 v142, v142\n\t"
    "v_exp_f32 v174, v174\n\t"
    "v_exp_f32 v143, v143\n\t"
    "v_exp_f32 v175, v175\n\t"
    "v_pk_add_f32 v[96:97], v[132:133], v[134:135]\n\t"
    "v_pk_add_f32 v[98:99], v[164:165], v[166:167]\n\t"
    "v_pk_add_f32 v[100:101], v[140:141], v[142:143]\n\t"
    "v_pk_add_f32 v[102:103], v[172:173], v[174:175]\n\t"
    "v_pk_add_f32 v[96:97], v[96:97], v[100:101]\n\t"
    "v_pk_add_f32 v[98:99], v[98:99], v[102:103]\n\t"
    "v_add_f32 v96, v96, v97\n\t"
    "v_add_f32 v98, v98, v99\n\t"
    "v_mov_b32 v100, v96\n\t"
    "v_mov_b32 v102, v98\n\t"
    "s_nop 0\n\t"
    "v_permlane16_swap_b32 v96, v100\n\t"
    "v_permlane16_swap_b32 v98, v102\n\t"
    "v_add_f32 v96, v96, v100\n\t"
    "v_add_f32 v98, v98, v102\n\t"
    "v_mov_b32 v100, v96\n\t"
    "v_mov_b32 v102, v98\n\t"
    "s_nop 0\n\t"
    "v_permlane32_swap_b32 v96, v100\n\t"
    "v_permlane32_swap_b32 v98, v102\n\t"
    "v_add_f32 v96, v96, v100\n\t"
    "v_add_f32 v98, v98, v102\n\t"
    "v_rcp_f32 v96, v96\n\t"
    "v_rcp_f32 v98, v98\n\t"
    "v_pk_mul_f32 v[132:133], v[132:133], v[96:97] op_sel_hi:[1,0]\n\t"
    "v_pk_mul_f32 v[134:135], v[134:135], v[96:97] op_sel_hi:[1,0]\n\t"
    "v_pk_mul_f32 v[140:141], v[140:141], v[96:97] op_sel_hi:[1,0]\n\t"
    "v_pk_mul_f32 v[142:143], v[142:143], v[96:97] op_sel_hi:[1,0]\n\t"
    "v_cvt_pk_bf16_f32 v56, v132, v133\n\t"
    "v_cvt_pk_bf16_f32 v57, v134, v135\n\t"
    "v_cvt_pk_bf16_f32 v58, v140, v141\n\t"
    "v_cvt_pk_bf16_f32 v59, v142, v143\n\t"
    "buffer_store_dwordx4 v[56:59], v14, s[32:35], s50 offen sc1\n\t"
    "v_pk_mul_f32 v[164:165], v[164:165], v[98:99] op_sel_hi:[1,0]\n\t"
    "v_pk_mul_f32 v[166:167], v[166:167], v[98:99] op_sel_hi:[1,0]\n\t"
    "v_pk_mul_f32 v[172:173], v[172:173], v[98:99] op_sel_hi:[1,0]\n\t"
    "v_pk_mul_f32 v[174:175], v[174:175], v[98:99] op_sel_hi:[1,0]\n\t"
    "v_cvt_pk_bf16_f32 v60, v164, v165\n\t"
    "v_cvt_pk_bf16_f32 v61, v166, v167\n\t"
    "v_cvt_pk_bf16_f32 v62, v172, v173\n\t"
    "v_cvt_pk_bf16_f32 v63, v174, v175\n\t"
    "buffer_store_dwordx4 v[60:63], v14, s[32:35], s50 offen offset:1024 sc1\n\t"
    "v_dot2_u32_u16 v8, v56, s84, v11\n\t"
    "v_dot2_u32_u16 v8, v57, s85, v8\n\t"
    "v_dot2_u32_u16 v8, v58, s86, v8\n\t"
    "v_dot2_u32_u16 v8, v59, s87, v8\n\t"
    "v_dot2_u32_u16 v8, v60, s88, v8\n\t"
    "v_dot2_u32_u16 v8, v61, s89, v8\n\t"
    "v_dot2_u32_u16 v8, v62, s90, v8\n\t"
    ".Lhop7_%=:\n\t"
    "s_cbranch_execz .Lhr_%=\n\t"
    "v_dot2_u32_u16 v8, v63, s91, v8\n\t"
    "s_mov_b32 s44, 0\n\t"
    "s_nop 1\n\t"
    "buffer_store_dword v8, v13, s[32:35], s50 offen offset:2048 sc1\n\t"
    "s_nop 0\n\t"
    "s_barrier\n\t"
    "ds_read_b128 v[64:67], v9 offset:0\n\t"
    "ds_read_b128 v[68:71], v10 offset:0\n\t"
    "ds_read_b128 v[72:75], v9 offset:2048\n\t"
    "ds_read_b128 v[76:79], v10 offset:2048\n\t"
    "ds_read_b64_tr_b16 v[16:17], v1 offset:16384\n\t"
    "ds_read_b64_tr_b16 v[18:19], v1 offset:16896\n\t"
    "ds_read_b64_tr_b16 v[20:21], v1 offset:20480\n\t"
    "ds_read_b64_tr_b16 v[22:23], v1 offset:20992\n\t"
    "ds_read_b64_tr_b16 v[24:25], v2 offset:16384\n\t"
    "ds_read_b64_tr_b16 v[26:27], v2 offset:16896\n\t"
    "ds_read_b64_tr_b16 v[28:29], v2 offset:20480\n\t"
    "ds_read_b64_tr_b16 v[30:31], v2 offset:20992\n\t"
    "ds_read_b64_tr_b16 v[32:33], v3 offset:16384\n\t"
    "ds_read_b64_tr_b16 v[34:35], v3 offset:16896\n\t"
    "ds_read_b64_tr_b16 v[36:37], v3 offset:20480\n\t"
    "ds_read_b64_tr_b16 v[38:39], v3 offset:20992\n\t"
    "s_waitcnt lgkmcnt(8)\n\t"
    "v_mfma_f32_16x16x32_bf16 v[128:131], v[64:67], v[16:19], 0\n\t"
    "ds_read_b64_tr_b16 v[40:41], v4 offset:16384\n\t"
    "v_mfma_f32_16x16x32_bf16 v[128:131], v[68:71], v[20:23], v[128:131]\n\t"
    "ds_read_b64_tr_b16 v[42:43], v4 offset:16896\n\t"
    "v_mfma_f32_16x16x32_bf16 v[132:135], v[72:75], v[16:19], 0\n\t"
    "ds_read_b64_tr_b16 v[44:45], v4 offset:20480\n\t"
    "v_mfma_f32_16x16x32_bf16 v[132:135], v[76:79], v[20:23], v[132:135]\n\t"
    "s_waitcnt lgkmcnt(7)\n\t"
    "ds_read_b64_tr_b16 v[46:47], v4 offset:20992\n\t"
    "v_mfma_f32_16x16x32_bf16 v[136:139], v[64:67], v[24:27], 0\n\t"
    "ds_read_b64_tr_b16 v[16:17], v1 offset:24576\n\t"
    "v_mfma_f32_16x16x32_bf16 v[136:139], v[68:71], v[28:31], v[136:139]\n\t"
    "ds_read_b64_tr_b16 v[18:19], v1 offset:25088\n\t"
    "v_mfma_f32_16x16x32_bf16 v[140:143], v[72:75], v[24:27], 0\n\t"
    "ds_read_b64_tr_b16 v[20:21], v1 offset:28672\n\t"
    "v_mfma_f32_16x16x32_bf16 v[140:143], v[76:79], v[28:31], v[140:143]\n\t"
    "s_waitcnt lgkmcnt(7)\n\t"
    "ds_read_b64_tr_b16 v[22:23], v1 offset:29184\n\t"
    "v_mfma_f32_16x16x32_bf16 v[144:147], v[64:67], v[32:35], 0\n\t"
    "ds_read_b64_tr_b16 v[24:25], v2 offset:24576\n\t"
    "v_mfma_f32_16x16x32_bf16 v[144:147], v[68:71], v[36:39], v[144:147]\n\t"
    "ds_read_b64_tr_b16 v[26:27], v2 offset:25088\n\t"
    "v_mfma_f32_16x16x32_bf16 v[148:151], v[72:75], v[32:35], 0\n\t"
    "ds_read_b64_tr_b16 v[28:29], v2 offset:28672\n\t"
    "v_mfma_f32_16x16x32_bf16 v[148:151], v[76:79], v[36:39], v[148:151]\n\t"
    "s_waitcnt lgkmcnt(7)\n\t"
    "ds_read_b64_tr_b16 v[30:31], v2 offset:29184\n\t"
    "v_mfma_f32_16x16x32_bf16 v[152:155], v[64:67], v[40:43], 0\n\t"
    "ds_read_b64_tr_b16 v[32:33], v3 offset:24576\n\t"
    "v_mfma_f32_16x16x32_bf16 v[152:155], v[68:71], v[44:47], v[152:155]\n\t"
    "ds_read_b64_tr_b16 v[34:35], v3 offset:25088\n\t"
    "v_mfma_f32_16x16x32_bf16 v[156:159], v[72:75], v[40:43], 0\n\t"
    "ds_read_b64_tr_b16 v[36:37], v3 offset:28672\n\t"
    "v_mfma_f32_16x16x32_bf16 v[156:159], v[76:79], v[44:47], v[156:159]\n\t"
    "s_waitcnt lgkmcnt(7)\n\t"
    "ds_read_b64_tr_b16 v[38:39], v3 offset:29184\n\t"
    "v_mfma_f32_16x16x32_bf16 v[160:163], v[64:67], v[16:19], 0\n\t"
    "ds_read_b64_tr_b16 v[40:41], v4 offset:24576\n\t"
    "v_mfma_f32_16x16x32_bf16 v[160:163], v[68:71], v[20:23], v[160:163]\n\t"
    "ds_read_b64_tr_b16 v[42:43], v4 offset:25088\n\t"
    "v_mfma_f32_16x16x32_bf16 v[164:167], v[72:75], v[16:19], 0\n\t"
    "ds_read_b64_tr_b16 v[44:45], v4 offset:28672\n\t"
    "v_mfma_f32_16x16x32_bf16 v[164:167], v[76:79], v[20:23], v[164:167]\n\t"
    "s_waitcnt lgkmcnt(7)\n\t"
    "ds_read_b64_tr_b16 v[46:47], v4 offset:29184\n\t"
    "v_mfma_f32_16x16x32_bf16 v[168:171], v[64:67], v[24:27], 0\n\t"
    "s_barrier\n\t"
    "ds_read_b64_tr_b16 v[16:17], v1 offset:49152\n\t"
    "v_mfma_f32_16x16x32_bf16 v[168:171], v[68:71], v[28:31], v[168:171]\n\t"
    "ds_read_b64_tr_b16 v[18:19], v1 offset:49664\n\t"
    "ds_read_b128 v[80:83], v9 offset:32768\n\t"
    "v_mfma_f32_16x16x32_bf16 v[172:175], v[72:75], v[24:27], 0\n\t"
    "ds_read_b64_tr_b16 v[20:21], v1 offset:53248\n\t"
    "v_mfma_f32_16x16x32_bf16 v[172:175], v[76:79], v[28:31], v[172:175]\n\t"
    "s_waitcnt lgkmcnt(8)\n\t"
    "ds_read_b64_tr_b16 v[22:23], v1 offset:53760\n\t"
    "ds_read_b128 v[84:87], v10 offset:32768\n\t"
    "v_mfma_f32_16x16x32_bf16 v[176:179], v[64:67], v[32:35], 0\n\t"
    "ds_read_b64_tr_b16 v[24:25], v2 offset:49152\n\t"
    "v_mfma_f32_16x16x32_bf16 v[176:179], v[68:71], v[36:39], v[176:179]\n\t"
    "ds_read_b64_tr_b16 v[26:27], v2 offset:49664\n\t"
    "ds_read_b128 v[88:91], v9 offset:34816\n\t"
    "v_mfma_f32_16x16x32_bf16 v[180:183], v[72:75], v[32:35], 0\n\t"
    "ds_read_b64_tr_b16 v[28:29], v2 offset:53248\n\t"
    "v_mfma_f32_16x16x32_bf16 v[180:183], v[76:79], v[36:39], v[180:183]\n\t"
    "s_waitcnt lgkmcnt(10)\n\t"
    "ds_read_b64_tr_b16 v[30:31], v2 offset:53760\n\t"
    "ds_read_b128 v[92:95], v10 offset:34816\n\t"
    "v_mfma_f32_16x16x32_bf16 v[184:187], v[64:67], v[40:43], 0\n\t"
    "ds_read_b64_tr_b16 v[32:33], v3 offset:49152\n\t"
    "v_mfma_f32_16x16x32_bf16 v[184:187], v[68:71], v[44:47], v[184:187]\n\t"
    "ds_read_b64_tr_b16 v[34:35], v3 offset:49664\n\t"
    "v_mfma_f32_16x16x32_bf16 v[188:191], v[72:75], v[40:43], 0\n\t"
    "ds_read_b64_tr_b16 v[36:37], v3 offset:53248\n\t"
    "v_mfma_f32_16x16x32_bf16 v[188:191], v[76:79], v[44:47], v[188:191]\n\t"
    "s_waitcnt lgkmcnt(3)\n\t"
    "ds_read_b64_tr_b16 v[38:39], v3 offset:53760\n\t"
    "v_mfma_f32_16x16x32_bf16 v[128:131], v[80:83], v[16:19], v[128:131]\n\t"
    "ds_read_b64_tr_b16 v[40:41], v4 offset:49152\n\t"
    "v_xor_b32 v9, 0x10000, v9\n\t"
    "v_mfma_f32_16x16x32_bf16 v[128:131], v[84:87], v[20:23], v[128:131]\n\t"
    "ds_read_b64_tr_b16 v[42:43], v4 offset:49664\n\t"
    "v_xor_b32 v10, 0x10000, v10\n\t"
    "v_mfma_f32_16x16x32_bf16 v[132:135], v[88:91], v[16:19], v[132:135]\n\t"
    "ds_read_b64_tr_b16 v[44:45], v4 offset:53248\n\t"
    "v_mfma_f32_16x16x32_bf16 v[132:135], v[92:95], v[20:23], v[132:135]\n\t"
    "ds_read_b64_tr_b16 v[46:47], v4 offset:53760\n\t"
    "v_mfma_f32_16x16x32_bf16 v[136:139], v[80:83], v[24:27], v[136:139]\n\t"
    "ds_read_b64_tr_b16 v[16:17], v1 offset:57344\n\t"
    "v_mfma_f32_16x16x32_bf16 v[136:139], v[84:87], v[28:31], v[136:139]\n\t"
    "ds_read_b64_tr_b16 v[18:19], v1 offset:57856\n\t"
    "v_cvt_pk_bf16_f32 v96, v128, v129\n\t"
    "v_mfma_f32_16x16x32_bf16 v[140:143], v[88:91], v[24:27], v[140:143]\n\t"
    "ds_read_b64_tr_b16 v[20:21], v1 offset:61440\n\t"
    "v_cvt_pk_bf16_f32 v97, v130, v131\n\t"
    "v_mfma_f32_16x16x32_bf16 v[140:143], v[92:95], v[28:31], v[140:143]\n\t"
    "s_waitcnt lgkmcnt(7)\n\t"
    "ds_read_b64_tr_b16 v[22:23], v1 offset:61952\n\t"
    "v_cvt_pk_bf16_f32 v98, v132, v133\n\t"
    "v_mfma_f32_16x16x32_bf16 v[144:147], v[80:83], v[32:35], v[144:147]\n\t"
    "ds_read_b64_tr_b16 v[24:25], v2 offset:57344\n\t"
    "v_cvt_pk_bf16_f32 v99, v134, v135\n\t"
    "v_mfma_f32_16x16x32_bf16 v[144:147], v[84:87], v[36:39], v[144:147]\n\t"
    "ds_read_b64_tr_b16 v[26:27], v2 offset:57856\n\t"
    "v_cvt_pk_bf16_f32 v100, v136, v137\n\t"
    "v_mfma_f32_16x16x32_bf16 v[148:151], v[88:91], v[32:35], v[148:151]\n\t"
    "ds_read_b64_tr_b16 v[28:29], v2 offset:61440\n\t"
    "v_cvt_pk_bf16_f32 v101, v138, v139\n\t"
    "v_mfma_f32_16x16x32_bf16 v[148:151], v[92:95], v[36:39], v[148:151]\n\t"
    "s_waitcnt lgkmcnt(7)\n\t"
    "ds_read_b64_tr_b16 v[30:31], v2 offset:61952\n\t"
    "v_cvt_pk_bf16_f32 v102, v140, v141\n\t"
    "v_mfma_f32_16x16x32_bf16 v[152:155], v[80:83], v[40:43], v[152:155]\n\t"
    ".Lhop8_%=:\n\t"
    "s_cbranch_execz .Lhr_%=\n\t"
    "ds_read_b64_tr_b16 v[32:33], v3 offset:57344\n\t"
    "v_cvt_pk_bf16_f32 v103, v142, v143\n\t"
    "v_mfma_f32_16x16x32_bf16 v[152:155], v[84:87], v[44:47], v[152:155]\n\t"
    "ds_read_b64_tr_b16 v[34:35], v3 offset:57856\n\t"
    "v_mfma_f32_16x16x32_bf16 v[156:159], v[88:91], v[40:43], v[156:159]\n\t"
    "ds_read_b64_tr_b16 v[36:37], v3 offset:61440\n\t"
    "v_xor_b32 v1, 0x10000, v1\n\t"
    "v_mfma_f32_16x16x32_bf16 v[156:159], v[92:95], v[44:47], v[156:159]\n\t"
    "s_waitcnt lgkmcnt(7)\n\t"
    "ds_read_b64_tr_b16 v[38:39], v3 offset:61952\n\t"
    "v_mfma_f32_16x16x32_bf16 v[160:163], v[80:83], v[16:19], v[160:163]\n\t"
    "s_barrier\n\t"
    "ds_read_b64_tr_b16 v[40:41], v4 offset:57344\n\t"
    "v_mfma_f32_16x16x32_bf16 v[160:163], v[84:87], v[20:23], v[160:163]\n\t"
    "ds_read_b64_tr_b16 v[42:43], v4 offset:57856\n\t"
    "v_mfma_f32_16x16x32_bf16 v[164:167], v[88:91], v[16:19], v[164:167]\n\t"
    "ds_read_b64_tr_b16 v[44:45], v4 offset:61440\n\t"
    "v_xor_b32 v2, 0x10000, v2\n\t"
    "v_mfma_f32_16x16x32_bf16 v[164:167], v[92:95], v[20:23], v[164:167]\n\t"
    "s_waitcnt lgkmcnt(7)\n\t"
    "ds_read_b64_tr_b16 v[46:47], v4 offset:61952\n\t"
    "v_mfma_f32_16x16x32_bf16 v[168:171], v[80:83], v[24:27], v[168:171]\n\t"
    "ds_read_b64_tr_b16 v[16:17], v1 offset:16384\n\t"
    "v_mfma_f32_16x16x32_bf16 v[168:171], v[84:87], v[28:31], v[168:171]\n\t"
    "ds_read_b64_tr_b16 v[18:19], v1 offset:16896\n\t"
    "ds_read_b128 v[64:67], v9 offset:0\n\t"
    "v_mfma_f32_16x16x32_bf16 v[172:175], v[88:91], v[24:27], v[172:175]\n\t"
    "ds_read_b64_tr_b16 v[20:21], v1 offset:20480\n\t"
    "v_mfma_f32_16x16x32_bf16 v[172:175], v[92:95], v[28:31], v[172:175]\n\t"
    "s_waitcnt lgkmcnt(8)\n\t"
    "ds_read_b64_tr_b16 v[22:23], v1 offset:20992\n\t"
    "ds_read_b128 v[68:71], v10 offset:0\n\t"
    "v_mfma_f32_16x16x32_bf16 v[176:179], v[80:83], v[32:35], v[176:179]\n\t"
    "ds_read_b64_tr_b16 v[24:25], v2 offset:16384\n\t"
    "v_mfma_f32_16x16x32_bf16 v[176:179], v[84:87], v[36:39], v[176:179]\n\t"
    "ds_read_b64_tr_b16 v[26:27], v2 offset:16896\n\t"
    "ds_read_b128 v[72:75], v9 offset:2048\n\t"
    "v_mfma_f32_16x16x32_bf16 v[180:183], v[88:91], v[32:35], v[180:183]\n\t"
    "ds_read_b64_tr_b16 v[28:29], v2 offset:20480\n\t"
    "v_xor_b32 v3, 0x10000, v3\n\t"
    "v_mfma_f32_16x16x32_bf16 v[180:183], v[92:95], v[36:39], v[180:183]\n\t"
    "s_waitcnt lgkmcnt(10)\n\t"
    "ds_read_b64_tr_b16 v[30:31], v2 offset:20992\n\t"
    "ds_read_b128 v[76:79], v10 offset:2048\n\t"
    "v_mfma_f32_16x16x32_bf16 v[184:187], v[80:83], v[40:43], v[184:187]\n\t"
    "ds_read_b64_tr_b16 v[32:33], v3 offset:16384\n\t"
    "v_xor_b32 v4, 0x10000, v4\n\t"
    "v_mfma_f32_16x16x32_bf16 v[184:187], v[84:87], v[44:47], v[184:187]\n\t"
    "ds_read_b64_tr_b16 v[34:35], v3 offset:16896\n\t"
    "v_mfma_f32_16x16x32_bf16 v[188:191], v[88:91], v[40:43], v[188:191]\n\t"
    "ds_read_b64_tr_b16 v[36:37], v3 offset:20480\n\t"
    "v_mfma_f32_16x16x32_bf16 v[188:191], v[92:95], v[44:47], v[188:191]\n\t"
    "s_waitcnt lgkmcnt(3)\n\t"
    "ds_read_b64_tr_b16 v[38:39], v3 offset:20992\n\t"
    ".Lp2_%=:\n\t"
    "v_mfma_f32_16x16x32_bf16 v[128:131], v[64:67], v[16:19], 0\n\t"
    "s_add_u32 s44, s44, 1\n\t"
    "ds_read_b64_tr_b16 v[40:41], v4 offset:16384\n\t"
    "v_cvt_pk_bf16_f32 v104, v144, v145\n\t"
    "v_mfma_f32_16x16x32_bf16 v[128:131], v[68:71], v[20:23], v[128:131]\n\t"
    "s_cmp_eq_u32 s44, 5\n\t"
    "ds_read_b64_tr_b16 v[42:43], v4 offset:16896\n\t"
    "v_cvt_pk_bf16_f32 v105, v146, v147\n\t"
    "v_mfma_f32_16x16x32_bf16 v[132:135], v[72:75], v[16:19], 0\n\t"
    "s_cselect_b64 s[82:83], -1, 0\n\t"
    "ds_read_b64_tr_b16 v[44:45], v4 offset:20480\n\t"
    "v_cvt_pk_bf16_f32 v106, v148, v149\n\t"
    "v_mfma_f32_16x16x32_bf16 v[132:135], v[76:79], v[20:23], v[132:135]\n\t"
    "ds_read_b64_tr_b16 v[46:47], v4 offset:20992\n\t"
    "v_cvt_pk_bf16_f32 v107, v150, v151\n\t"
    "v_mfma_f32_16x16x32_bf16 v[136:139], v[64:67], v[24:27], 0\n\t"
    "ds_read_b64_tr_b16 v[16:17], v1 offset:24576\n\t"
    "v_cvt_pk_bf16_f32 v108, v152, v153\n\t"
    "v_mfma_f32_16x16x32_bf16 v[136:139], v[68:71], v[28:31], v[136:139]\n\t"
    "ds_read_b64_tr_b16 v[18:19], v1 offset:25088\n\t"
    "v_cvt_pk_bf16_f32 v109, v154, v155\n\t"
    "v_mfma_f32_16x16x32_bf16 v[140:143], v[72:75], v[24:27], 0\n\t"
    "ds_read_b64_tr_b16 v[20:21], v1 offset:28672\n\t"
    "v_cvt_pk_bf16_f32 v110, v156, v157\n\t"
    "v_mfma_f32_16x16x32_bf16 v[140:143], v[76:79], v[28:31], v[140:143]\n\t"
    "s_waitcnt lgkmcnt(7)\n\t"
    "ds_read_b64_tr_b16 v[22:23], v1 offset:29184\n\t"
    "v_cvt_pk_bf16_f32 v111, v158, v159\n\t"
    "v_mfma_f32_16x16x32_bf16 v[144:147], v[64:67], v[32:35], 0\n\t"
    "ds_read_b64_tr_b16 v[24:25], v2 offset:24576\n\t"
    "v_cvt_pk_bf16_f32 v112, v160, v161\n\t"
    "v_mfma_f32_16x16x32_bf16 v[144:147], v[68:71], v[36:39], v[144:147]\n\t"
    "ds_read_b64_tr_b16 v[26:27], v2 offset:25088\n\t"
    "v_cvt_pk_bf16_f32 v113, v162, v163\n\t"
    "v_mfma_f32_16x16x32_bf16 v[148:151], v[72:75], v[32:35], 0\n\t"
    "ds_read_b64_tr_b16 v[28:29], v2 offset:28672\n\t"
    "v_cvt_pk_bf16_f32 v114, v164, v165\n\t"
    "v_mfma_f32_16x16x32_bf16 v[148:151], v[76:79], v[36:39], v[148:151]\n\t"
    "s_waitcnt lgkmcnt(7)\n\t"
    "ds_read_b64_tr_b16 v[30:31], v2 offset:29184\n\t"
    "v_cvt_pk_bf16_f32 v115, v166, v167\n\t"
    "v_mfma_f32_16x16x32_bf16 v[152:155], v[64:67], v[40:43], 0\n\t"
    "s_barrier\n\t"
    "ds_read_b64_tr_b16 v[32:33], v3 offset:24576\n\t"
    "v_mfma_f32_16x16x32_bf16 v[152:155], v[68:71], v[44:47], v[152:155]\n\t"
    "ds_read_b64_tr_b16 v[34:35], v3 offset:25088\n\t"
    "v_cvt_pk_bf16_f32 v116, v168, v169\n\t"
    "v_mfma_f32_16x16x32_bf16 v[156:159], v[72:75], v[40:43], 0\n\t"
    "ds_read_b64_tr_b16 v[36:37], v3 offset:28672\n\t"
    "v_cvt_pk_bf16_f32 v117, v170, v171\n\t"
    "v_mfma_f32_16x16x32_bf16 v[156:159], v[76:79], v[44:47], v[156:159]\n\t"
    "s_waitcnt lgkmcnt(7)\n\t"
    "ds_read_b64_tr_b16 v[38:39], v3 offset:29184\n\t"
    "v_cvt_pk_bf16_f32 v118, v172, v173\n\t"
    "v_mfma_f32_16x16x32_bf16 v[160:163], v[64:67], v[16:19], 0\n\t"
    "ds_read_b64_tr_b16 v[40:41], v4 offset:24576\n\t"
    "v_cvt_pk_bf16_f32 v119, v174, v175\n\t"
    "v_mfma_f32_16x16x32_bf16 v[160:163], v[68:71], v[20:23], v[160:163]\n\t"
    "ds_read_b64_tr_b16 v[42:43], v4 offset:25088\n\t"
    "v_cvt_pk_bf16_f32 v120, v176, v177\n\t"
    "v_mfma_f32_16x16x32_bf16 v[164:167], v[72:75], v[16:19], 0\n\t"
    "ds_read_b64_tr_b16 v[44:45], v4 offset:28672\n\t"
    "v_cvt_pk_bf16_f32 v121, v178, v179\n\t"
    "v_mfma_f32_16x16x32_bf16 v[164:167], v[76:79], v[20:23], v[164:167]\n\t"
    "s_waitcnt lgkmcnt(7)\n\t"
    "ds_read_b64_tr_b16 v[46:47], v4 offset:29184\n\t"
    "v_cvt_pk_bf16_f32 v122, v180, v181\n\t"
    "v_mfma_f32_16x16x32_bf16 v[168:171], v[64:67], v[24:27], 0\n\t"
    "ds_read_b64_tr_b16 v[16:17], v1 offset:49152\n\t"
    "v_cvt_pk_bf16_f32 v123, v182, v183\n\t"
    "v_mfma_f32_16x16x32_bf16 v[168:171], v[68:71], v[28:31], v[168:171]\n\t"
    "ds_read_b64_tr_b16 v[18:19], v1 offset:49664\n\t"
    "v_cvt_pk_bf16_f32 v124, v184, v185\n\t"
    "v_mfma_f32_16x16x32_bf16 v[192:195], v[96:99], v[56:59], v[192:195]\n\t"
    "ds_read_b128 v[80:83], v9 offset:32768\n\t"
    "v_cvt_pk_bf16_f32 v125, v186, v187\n\t"
    "v_mfma_f32_16x16x32_bf16 v[172:175], v[72:75], v[24:27], 0\n\t"
    "ds_read_b64_tr_b16 v[20:21], v1 offset:53248\n\t"
    "v_cvt_pk_bf16_f32 v126, v188, v189\n\t"
    "v_mfma_f32_16x16x32_bf16 v[172:175], v[76:79], v[28:31], v[172:175]\n\t"
    ".Lhop9_%=:\n\t"
    "s_cbranch_execz .Lhr_%=\n\t"
    "ds_read_b64_tr_b16 v[22:23], v1 offset:53760\n\t"
    "v_cvt_pk_bf16_f32 v127, v190, v191\n\t"
    "v_mfma_f32_16x16x32_bf16 v[200:203], v[96:99], v[60:63], v[200:203]\n\t"
    "s_waitcnt lgkmcnt(9)\n\t"
    "ds_read_b128 v[84:87], v10 offset:32768\n\t"
    "v_mov_b64 v[48:49], v[56:57]\n\t"
    "v_mfma_f32_16x16x32_bf16 v[176:179], v[64:67], v[32:35], 0\n\t"
    "ds_read_b64_tr_b16 v[24:25], v2 offset:49152\n\t"
    "v_mov_b64 v[50:51], v[58:59]\n\t"
    "v_mfma_f32_16x16x32_bf16 v[176:179], v[68:71], v[36:39], v[176:179]\n\t"
    "ds_read_b64_tr_b16 v[26:27], v2 offset:49664\n\t"
    "v_mov_b64 v[52:53], v[60:61]\n\t"
    "v_mfma_f32_16x16x32_bf16 v[196:199], v[100:103], v[56:59], v[196:199]\n\t"
    "ds_read_b128 v[88:91], v9 offset:34816\n\t"
    "v_mov_b64 v[54:55], v[62:63]\n\t"
    "v_mfma_f32_16x16x32_bf16 v[180:183], v[72:75], v[32:35], 0\n\t"
    "ds_read_b64_tr_b16 v[28:29], v2 offset:53248\n\t"
    "v_xor_b32 v6, 0x4000, v6\n\t"
    "v_mfma_f32_16x16x32_bf16 v[180:183], v[76:79], v[36:39], v[180:183]\n\t"
    "ds_read_b64_tr_b16 v[30:31], v2 offset:53760\n\t"
    "v_xor_b32 v7, 0x4000, v7\n\t"
    "v_mfma_f32_16x16x32_bf16 v[204:207], v[100:103], v[60:63], v[204:207]\n\t"
    "s_waitcnt lgkmcnt(11)\n\t"
    "ds_read_b128 v[92:95], v10 offset:34816\n\t"
    "v_mfma_f32_16x16x32_bf16 v[184:187], v[64:67], v[40:43], 0\n\t"
    "ds_read_b64_tr_b16 v[32:33], v3 offset:49152\n\t"
    "v_mfma_f32_16x16x32_bf16 v[184:187], v[68:71], v[44:47], v[184:187]\n\t"
    "ds_read_b64_tr_b16 v[34:35], v3 offset:49664\n\t"
    "v_mfma_f32_16x16x32_bf16 v[188:191], v[72:75], v[40:43], 0\n\t"
    "ds_read_b64_tr_b16 v[36:37], v3 offset:53248\n\t"
    "v_mfma_f32_16x16x32_bf16 v[188:191], v[76:79], v[44:47], v[188:191]\n\t"
    "s_waitcnt lgkmcnt(3)\n\t"
    "ds_read_b64_tr_b16 v[38:39], v3 offset:53760\n\t"
    "v_mfma_f32_16x16x32_bf16 v[128:131], v[80:83], v[16:19], v[128:131]\n\t"
    "ds_read_b64_tr_b16 v[40:41], v4 offset:49152\n\t"
    "v_xor_b32 v9, 0x10000, v9\n\t"
    "v_mfma_f32_16x16x32_bf16 v[128:131], v[84:87], v[20:23], v[128:131]\n\t"
    "ds_read_b64_tr_b16 v[42:43], v4 offset:49664\n\t"
    "v_xor_b32 v10, 0x10000, v10\n\t"
    "v_mfma_f32_16x16x32_bf16 v[132:135], v[88:91], v[16:19], v[132:135]\n\t"
    "ds_read_b64_tr_b16 v[44:45], v4 offset:53248\n\t"
    "v_mfma_f32_16x16x32_bf16 v[132:135], v[92:95], v[20:23], v[132:135]\n\t"
    "ds_read_b64_tr_b16 v[46:47], v4 offset:53760\n\t"
    "v_mfma_f32_16x16x32_bf16 v[136:139], v[80:83], v[24:27], v[136:139]\n\t"
    "ds_read_b64_tr_b16 v[16:17], v1 offset:57344\n\t"
    "v_mfma_f32_16x16x32_bf16 v[136:139], v[84:87], v[28:31], v[136:139]\n\t"
    "ds_read_b64_tr_b16 v[18:19], v1 offset:57856\n\t"
    "v_cvt_pk_bf16_f32 v96, v128, v129\n\t"
    "v_mfma_f32_16x16x32_bf16 v[208:211], v[104:107], v[48:51], v[208:211]\n\t"
    "v_cvt_pk_bf16_f32 v97, v130, v131\n\t"
    "v_mfma_f32_16x16x32_bf16 v[140:143], v[88:91], v[24:27], v[140:143]\n\t"
    "ds_read_b64_tr_b16 v[20:21], v1 offset:61440\n\t"
    "v_cvt_pk_bf16_f32 v98, v132, v133\n\t"
    "v_mfma_f32_16x16x32_bf16 v[140:143], v[92:95], v[28:31], v[140:143]\n\t"
    "ds_read_b64_tr_b16 v[22:23], v1 offset:61952\n\t"
    "v_cvt_pk_bf16_f32 v99, v134, v135\n\t"
    "v_mfma_f32_16x16x32_bf16 v[216:219], v[104:107], v[52:55], v[216:219]\n\t"
    "s_waitcnt lgkmcnt(8)\n\t"
    "v_cvt_pk_bf16_f32 v100, v136, v137\n\t"
    "v_mfma_f32_16x16x32_bf16 v[144:147], v[80:83], v[32:35], v[144:147]\n\t"
    "ds_read_b64_tr_b16 v[24:25], v2 offset:57344\n\t"
    "v_cvt_pk_bf16_f32 v101, v138, v139\n\t"
    "v_mfma_f32_16x16x32_bf16 v[144:147], v[84:87], v[36:39], v[144:147]\n\t"
    "ds_read_b64_tr_b16 v[26:27], v2 offset:57856\n\t"
    "v_cvt_pk_bf16_f32 v102, v140, v141\n\t"
    "v_mfma_f32_16x16x32_bf16 v[212:215], v[108:111], v[48:51], v[212:215]\n\t"
    "s_barrier\n\t"
    "v_mfma_f32_16x16x32_bf16 v[148:151], v[88:91], v[32:35], v[148:151]\n\t"
    "ds_read_b64_tr_b16 v[28:29], v2 offset:61440\n\t"
    "v_cvt_pk_bf16_f32 v103, v142, v143\n\t"
    "v_mfma_f32_16x16x32_bf16 v[148:151], v[92:95], v[36:39], v[148:151]\n\t"
    "ds_read_b64_tr_b16 v[30:31], v2 offset:61952\n\t"
    "v_mfma_f32_16x16x32_bf16 v[220:223], v[108:111], v[52:55], v[220:223]\n\t"
    "s_waitcnt lgkmcnt(8)\n\t"
    "ds_read_b128 v[56:59], v6\n\t"
    "v_mfma_f32_16x16x32_bf16 v[152:155], v[80:83], v[40:43], v[152:155]\n\t"
    "ds_read_b64_tr_b16 v[32:33], v3 offset:57344\n\t"
    "v_mfma_f32_16x16x32_bf16 v[152:155], v[84:87], v[44:47], v[152:155]\n\t"
    "ds_read_b64_tr_b16 v[34:35], v3 offset:57856\n\t"
    "v_mfma_f32_16x16x32_bf16 v[224:227], v[112:115], v[48:51], v[224:227]\n\t"
    "ds_read_b128 v[60:63], v6 offset:1024\n\t"
    "v_mfma_f32_16x16x32_bf16 v[156:159], v[88:91], v[40:43], v[156:159]\n\t"
    "ds_read_b64_tr_b16 v[36:37], v3 offset:61440\n\t"
    "v_xor_b32 v1, 0x10000, v1\n\t"
    "v_mfma_f32_16x16x32_bf16 v[156:159], v[92:95], v[44:47], v[156:159]\n\t"
    "ds_read_b64_tr_b16 v[38:39], v3 offset:61952\n\t"
    "v_mfma_f32_16x16x32_bf16 v[232:235], v[112:115], v[52:55], v[232:235]\n\t"
    "s_waitcnt lgkmcnt(5)\n\t"
    "ds_read_b32 v8, v7 offset:2048\n\t"
    "v_mad_u32_u16 v11, v56, s84, 0\n\t"
    "v_mfma_f32_16x16x32_bf16 v[160:163], v[80:83], v[16:19], v[160:163]\n\t"
    "ds_read_b64_tr_b16 v[40:41], v4 offset:57344\n\t"
    "v_mad_u32_u16 v11, v56, s84, v11 op_sel:[1,1,0,0]\n\t"
    "v_mfma_f32_16x16x32_bf16 v[160:163], v[84:87], v[20:23], v[160:163]\n\t"
    "ds_read_b64_tr_b16 v[42:43], v4 offset:57856\n\t"
    "v_mad_u32_u16 v11, v57, s85, v11\n\t"
    "v_mfma_f32_16x16x32_bf16 v[228:231], v[116:119], v[48:51], v[228:231]\n\t"
    "v_mad_u32_u16 v11, v57, s85, v11 op_sel:[1,1,0,0]\n\t"
    "v_mfma_f32_16x16x32_bf16 v[164:167], v[88:91], v[16:19], v[164:167]\n\t"
    "ds_read_b64_tr_b16 v[44:45], v4 offset:61440\n\t"
    "v_xor_b32 v2, 0x10000, v2\n\t"
    "v_mfma_f32_16x16x32_bf16 v[164:167], v[92:95], v[20:23], v[164:167]\n\t"
    "ds_read_b64_tr_b16 v[46:47], v4 offset:61952\n\t"
    "v_mad_u32_u16 v11, v58, s86, v11\n\t"
    "v_mfma_f32_16x16x32_bf16 v[236:239], v[116:119], v[52:55], v[236:239]\n\t"
    "v_mad_u32_u16 v11, v58, s86, v11 op_sel:[1,1,0,0]\n\t"
    "v_mfma_f32_16x16x32_bf16 v[168:171], v[80:83], v[24:27], v[168:171]\n\t"
    "ds_read_b64_tr_b16 v[16:17], v1 offset:16384\n\t"
    "v_mad_u32_u16 v11, v59, s87, v11\n\t"
    "v_mfma_f32_16x16x32_bf16 v[168:171], v[84:87], v[28:31], v[168:171]\n\t"
    "ds_read_b64_tr_b16 v[18:19], v1 offset:16896\n\t"
    "v_mad_u32_u16 v11, v59, s87, v11 op_sel:[1,1,0,0]\n\t"
    "v_mfma_f32_16x16x32_bf16 v[240:243], v[120:123], v[48:51], v[240:243]\n\t"
    "s_waitcnt lgkmcnt(9)\n\t"
    "ds_read_b128 v[64:67], v9 offset:0\n\t"
    "v_mad_u32_u16 v11, v60, s88, v11\n\t"
    "v_mfma_f32_16x16x32_bf16 v[172:175], v[88:91], v[24:27], v[172:175]\n\t"
    "ds_read_b64_tr_b16 v[20:21], v1 offset:20480\n\t"
    "v_mad_u32_u16 v11, v60, s88, v11 op_sel:[1,1,0,0]\n\t"
    "v_mfma_f32_16x16x32_bf16 v[172:175], v[92:95], v[28:31], v[172:175]\n\t"
    "ds_read_b64_tr_b16 v[22:23], v1 offset:20992\n\t"
    "v_mad_u32_u16 v11, v61, s89, v11\n\t"
    "v_mfma_f32_16x16x32_bf16 v[248:251], v[120:123], v[52:55], v[248:251]\n\t"
    "s_waitcnt lgkmcnt(10)\n\t"
    "ds_read_b128 v[68:71], v10 offset:0\n\t"
    "v_mad_u32_u16 v11, v61, s89, v11 op_sel:[1,1,0,0]\n\t"
    "v_mfma_f32_16x16x32_bf16 v[176:179], v[80:83], v[32:35], v[176:179]\n\t"
    "ds_read_b64_tr_b16 v[24:25], v2 offset:16384\n\t"
    "v_mad_u32_u16 v11, v62, s90, v11\n\t"
    "v_mfma_f32_16x16x32_bf16 v[176:179], v[84:87], v[36:39], v[176:179]\n\t"
    "ds_read_b64_tr_b16 v[26:27], v2 offset:16896\n\t"
    "v_mad_u32_u16 v11, v62, s90, v11 op_sel:[1,1,0,0]\n\t"
    "v_mfma_f32_16x16x32_bf16 v[244:247], v[124:127], v[48:51], v[244:247]\n\t"
    ".Lhop10_%=:\n\t"
    "s_cbranch_execz .Lhr_%=\n\t"
    "ds_read_b128 v[72:75], v9 offset:2048\n\t"
    "v_mad_u32_u16 v11, v63, s91, v11\n\t"
    "v_mfma_f32_16x16x32_bf16 v[180:183], v[88:91], v[32:35], v[180:183]\n\t"
    "ds_read_b64_tr_b16 v[28:29], v2 offset:20480\n\t"
    "v_xor_b32 v3, 0x10000, v3\n\t"
    "v_mfma_f32_16x16x32_bf16 v[180:183], v[92:95], v[36:39], v[180:183]\n\t"
    "ds_read_b64_tr_b16 v[30:31], v2 offset:20992\n\t"
    "v_mad_u32_u16 v11, v63, s91, v11 op_sel:[1,1,0,0]\n\t"
    "v_mfma_f32_16x16x32_bf16 v[252:255], v[124:127], v[52:55], v[252:255]\n\t"
    "s_waitcnt lgkmcnt(11)\n\t"
    "ds_read_b128 v[76:79], v10 offset:2048\n\t"
    "v_sub_u32 v11, v8, v11\n\t"
    "v_mfma_f32_16x16x32_bf16 v[184:187], v[80:83], v[40:43], v[184:187]\n\t"
    "ds_read_b64_tr_b16 v[32:33], v3 offset:16384\n\t"
    "v_xor_b32 v4, 0x10000, v4\n\t"
    "v_mfma_f32_16x16x32_bf16 v[184:187], v[84:87], v[44:47], v[184:187]\n\t"
    "ds_read_b64_tr_b16 v[34:35], v3 offset:16896\n\t"
    "v_cmp_ne_u32 vcc, s46, v11\n\t"
    "v_mfma_f32_16x16x32_bf16 v[188:191], v[88:91], v[40:43], v[188:191]\n\t"
    "s_or_b64 s[78:79], vcc, s[82:83]\n\t"
    "ds_read_b64_tr_b16 v[36:37], v3 offset:20480\n\t"
    "v_mfma_f32_16x16x32_bf16 v[188:191], v[92:95], v[44:47], v[188:191]\n\t"
    "s_waitcnt lgkmcnt(3)\n\t"
    "ds_read_b64_tr_b16 v[38:39], v3 offset:20992\n\t"
    "s_cbranch_scc0 .Lp2_%=\n\t"
    "s_cbranch_vccnz .Lrt_%=\n\t"
    ".Ldr_%=:\n\t"
    "s_barrier\n\t"
    "v_mfma_f32_16x16x32_bf16 v[128:131], v[64:67], v[16:19], 0\n\t"
    "ds_read_b64_tr_b16 v[40:41], v1 offset:49152\n\t"
    "ds_read_b128 v[80:83], v9 offset:32768\n\t"
    "v_mfma_f32_16x16x32_bf16 v[128:131], v[68:71], v[20:23], v[128:131]\n\t"
    "ds_read_b64_tr_b16 v[42:43], v1 offset:49664\n\t"
    "ds_read_b128 v[84:87], v10 offset:32768\n\t"
    "v_mfma_f32_16x16x32_bf16 v[192:195], v[96:99], v[56:59], v[192:195]\n\t"
    "ds_read_b128 v[88:91], v9 offset:34816\n\t"
    "v_cvt_pk_bf16_f32 v104, v144, v145\n\t"
    "v_mfma_f32_16x16x32_bf16 v[132:135], v[72:75], v[16:19], 0\n\t"
    "ds_read_b64_tr_b16 v[44:45], v1 offset:53248\n\t"
    "ds_read_b128 v[92:95], v10 offset:34816\n\t"
    "v_mfma_f32_16x16x32_bf16 v[132:135], v[76:79], v[20:23], v[132:135]\n\t"
    "ds_read_b64_tr_b16 v[46:47], v1 offset:53760\n\t"
    "v_xor_b32 v1, 0x10000, v1\n\t"
    "v_mfma_f32_16x16x32_bf16 v[200:203], v[96:99], v[60:63], v[200:203]\n\t"
    "v_cvt_pk_bf16_f32 v105, v146, v147\n\t"
    "v_cvt_pk_bf16_f32 v106, v148, v149\n\t"
    "v_mfma_f32_16x16x32_bf16 v[136:139], v[64:67], v[24:27], 0\n\t"
    "ds_read_b64_tr_b16 v[16:17], v2 offset:49152\n\t"
    "v_cvt_pk_bf16_f32 v107, v150, v151\n\t"
    "v_mfma_f32_16x16x32_bf16 v[136:139], v[68:71], v[28:31], v[136:139]\n\t"
    "ds_read_b64_tr_b16 v[18:19], v2 offset:49664\n\t"
    "v_xor_b32 v6, 0x4000, v6\n\t"
    "v_mfma_f32_16x16x32_bf16 v[196:199], v[100:103], v[56:59], v[196:199]\n\t"
    "ds_read_b128 v[48:51], v6\n\t"
    "v_xor_b32 v7, 0x4000, v7\n\t"
    "v_mfma_f32_16x16x32_bf16 v[140:143], v[72:75], v[24:27], 0\n\t"
    "ds_read_b64_tr_b16 v[20:21], v2 offset:53248\n\t"
    "ds_read_b128 v[52:55], v6 offset:1024\n\t"
    "v_mfma_f32_16x16x32_bf16 v[140:143], v[76:79], v[28:31], v[140:143]\n\t"
    "ds_read_b64_tr_b16 v[22:23], v2 offset:53760\n\t"
    "ds_read_b32 v8, v7 offset:2048\n\t"
    "v_xor_b32 v2, 0x10000, v2\n\t"
    "v_mfma_f32_16x16x32_bf16 v[204:207], v[100:103], v[60:63], v[204:207]\n\t"
    "s_waitcnt lgkmcnt(15)\n\t"
    "v_cvt_pk_bf16_f32 v108, v152, v153\n\t"
    "v_cvt_pk_bf16_f32 v109, v154, v155\n\t"
    "v_mfma_f32_16x16x32_bf16 v[144:147], v[64:67], v[32:35], 0\n\t"
    "ds_read_b64_tr_b16 v[24:25], v4 offset:16384\n\t"
    "v_cvt_pk_bf16_f32 v110, v156, v157\n\t"
    "v_mfma_f32_16x16x32_bf16 v[144:147], v[68:71], v[36:39], v[144:147]\n\t"
    "ds_read_b64_tr_b16 v[26:27], v4 offset:16896\n\t"
    "v_cvt_pk_bf16_f32 v111, v158, v159\n\t"
    "v_mfma_f32_16x16x32_bf16 v[208:211], v[104:107], v[56:59], v[208:211]\n\t"
    "v_cvt_pk_bf16_f32 v112, v160, v161\n\t"
    "v_cvt_pk_bf16_f32 v113, v162, v163\n\t"
    "v_mfma_f32_16x16x32_bf16 v[148:151], v[72:75], v[32:35], 0\n\t"
    "ds_read_b64_tr_b16 v[28:29], v4 offset:20480\n\t"
    "v_cvt_pk_bf16_f32 v114, v164, v165\n\t"
    "v_mfma_f32_16x16x32_bf16 v[148:151], v[76:79], v[36:39], v[148:151]\n\t"
    "ds_read_b64_tr_b16 v[30:31], v4 offset:20992\n\t"
    "v_cvt_pk_bf16_f32 v115, v166, v167\n\t"
    "v_mov_b32 v11, s46\n\t"
    "s_waitcnt lgkmcnt(8)\n\t"
    "v_dot2_u32_u16 v11, v48, s84, v11\n\t"
    "v_dot2_u32_u16 v11, v49, s85, v11\n\t"
    "v_dot2_u32_u16 v11, v50, s86, v11\n\t"
    "v_dot2_u32_u16 v11, v51, s87, v11\n\t"
    "s_waitcnt lgkmcnt(6)\n\t"
    "v_dot2_u32_u16 v11, v52, s88, v11\n\t"
    "v_dot2_u32_u16 v11, v53, s89, v11\n\t"
    "v_dot2_u32_u16 v11, v54, s90, v11\n\t"
    "v_dot2_u32_u16 v11, v55, s91, v11\n\t"
    "s_nop 2\n\t"
    "s_waitcnt lgkmcnt(4)\n\t"
    "v_cmp_ne_u32 vcc, v8, v11\n\t"
    "s_cbranch_vccnz .Lr6_%=\n\t"
    ".Lr6x_%=:\n\t"
    "v_mfma_f32_16x16x32_bf16 v[216:219], v[104:107], v[60:63], v[216:219]\n\t"
    "v_cvt_pk_bf16_f32 v116, v168, v169\n\t"
    "v_cvt_pk_bf16_f32 v117, v170, v171\n\t"
    "v_mfma_f32_16x16x32_bf16 v[128:131], v[80:83], v[40:43], v[128:131]\n\t"
    "ds_read_b64_tr_b16 v[32:33], v3 offset:49152\n\t"
    "v_cvt_pk_bf16_f32 v118, v172, v173\n\t"
    "v_mfma_f32_16x16x32_bf16 v[128:131], v[84:87], v[44:47], v[128:131]\n\t"
    "ds_read_b64_tr_b16 v[34:35], v3 offset:49664\n\t"
    "v_cvt_pk_bf16_f32 v119, v174, v175\n\t"
    "v_mfma_f32_16x16x32_bf16 v[132:135], v[88:91], v[40:43], v[132:135]\n\t"
    "ds_read_b64_tr_b16 v[36:37], v3 offset:53248\n\t"
    "v_xor_b32 v9, 0x10000, v9\n\t"
    "v_mfma_f32_16x16x32_bf16 v[132:135], v[92:95], v[44:47], v[132:135]\n\t"
    "ds_read_b64_tr_b16 v[38:39], v3 offset:53760\n\t"
    "ds_read_b128 v[160:163], v9 offset:0\n\t"
    "v_xor_b32 v3, 0x10000, v3\n\t"
    "v_mfma_f32_16x16x32_bf16 v[136:139], v[80:83], v[16:19], v[136:139]\n\t"
    "ds_read_b64_tr_b16 v[40:41], v4 offset:49152\n\t"
    "ds_read_b128 v[168:171], v9 offset:2048\n\t"
    "v_mfma_f32_16x16x32_bf16 v[136:139], v[84:87], v[20:23], v[136:139]\n\t"
    "ds_read_b64_tr_b16 v[42:43], v4 offset:49664\n\t"
    "v_xor_b32 v10, 0x10000, v10\n\t"
    "v_mfma_f32_16x16x32_bf16 v[140:143], v[88:91], v[16:19], v[140:143]\n\t"
    "ds_read_b64_tr_b16 v[44:45], v4 offset:53248\n\t"
    "ds_read_b128 v[164:167], v10 offset:0\n\t"
    "v_mfma_f32_16x16x32_bf16 v[140:143], v[92:95], v[20:23], v[140:143]\n\t"
    "s_waitcnt lgkmcnt(10)\n\t"
    "ds_read_b64_tr_b16 v[46:47], v4 offset:53760\n\t"
    "ds_read_b128 v[172:175], v10 offset:2048\n\t"
    "v_xor_b32 v4, 0x10000, v4\n\t"
    "v_mfma_f32_16x16x32_bf16 v[152:155], v[64:67], v[24:27], 0\n\t"
    "s_barrier\n\t"
    "ds_read_b64_tr_b16 v[16:17], v1 offset:16384\n\t"
    "v_cvt_pk_bf16_f32 v120, v176, v177\n\t"
    "v_mfma_f32_16x16x32_bf16 v[152:155], v[68:71], v[28:31], v[152:155]\n\t"
    "ds_read_b64_tr_b16 v[18:19], v1 offset:16896\n\t"
    "v_cvt_pk_bf16_f32 v121, v178, v179\n\t"
    "v_mfma_f32_16x16x32_bf16 v[212:215], v[108:111], v[56:59], v[212:215]\n\t"
    "v_cvt_pk_bf16_f32 v122, v180, v181\n\t"
    "v_cvt_pk_bf16_f32 v123, v182, v183\n\t"
    "v_mfma_f32_16x16x32_bf16 v[156:159], v[72:75], v[24:27], 0\n\t"
    ".Lhop11_%=:\n\t"
    "s_cbranch_execz .Lhr_%=\n\t"
    "ds_read_b64_tr_b16 v[20:21], v1 offset:20480\n\t"
    "v_cvt_pk_bf16_f32 v124, v184, v185\n\t"
    "v_mfma_f32_16x16x32_bf16 v[156:159], v[76:79], v[28:31], v[156:159]\n\t"
    "ds_read_b64_tr_b16 v[22:23], v1 offset:20992\n\t"
    "v_cvt_pk_bf16_f32 v125, v186, v187\n\t"
    "v_mfma_f32_16x16x32_bf16 v[220:223], v[108:111], v[60:63], v[220:223]\n\t"
    "s_waitcnt lgkmcnt(12)\n\t"
    "v_cvt_pk_bf16_f32 v126, v188, v189\n\t"
    "v_cvt_pk_bf16_f32 v127, v190, v191\n\t"
    "v_mfma_f32_16x16x32_bf16 v[144:147], v[80:83], v[32:35], v[144:147]\n\t"
    "ds_read_b64_tr_b16 v[24:25], v2 offset:16384\n\t"
    "v_cvt_pk_bf16_f32 v128, v128, v129\n\t"
    "v_mfma_f32_16x16x32_bf16 v[144:147], v[84:87], v[36:39], v[144:147]\n\t"
    "ds_read_b64_tr_b16 v[26:27], v2 offset:16896\n\t"
    "v_cvt_pk_bf16_f32 v129, v130, v131\n\t"
    "v_mfma_f32_16x16x32_bf16 v[148:151], v[88:91], v[32:35], v[148:151]\n\t"
    "ds_read_b64_tr_b16 v[28:29], v2 offset:20480\n\t"
    "v_cvt_pk_bf16_f32 v130, v132, v133\n\t"
    "v_mfma_f32_16x16x32_bf16 v[148:151], v[92:95], v[36:39], v[148:151]\n\t"
    "s_waitcnt lgkmcnt(8)\n\t"
    "ds_read_b64_tr_b16 v[30:31], v2 offset:20992\n\t"
    "v_cvt_pk_bf16_f32 v131, v134, v135\n\t"
    "v_mfma_f32_16x16x32_bf16 v[152:155], v[80:83], v[40:43], v[152:155]\n\t"
    "s_barrier\n\t"
    "ds_read_b64_tr_b16 v[32:33], v1 offset:49152\n\t"
    "v_cvt_pk_bf16_f32 v136, v136, v137\n\t"
    "v_mfma_f32_16x16x32_bf16 v[152:155], v[84:87], v[44:47], v[152:155]\n\t"
    "ds_read_b64_tr_b16 v[34:35], v1 offset:49664\n\t"
    "ds_read_b128 v[176:179], v9 offset:32768\n\t"
    "v_mfma_f32_16x16x32_bf16 v[192:195], v[128:131], v[48:51], v[192:195]\n\t"
    "ds_read_b128 v[180:183], v10 offset:32768\n\t"
    "v_cvt_pk_bf16_f32 v137, v138, v139\n\t"
    "v_mfma_f32_16x16x32_bf16 v[200:203], v[128:131], v[52:55], v[200:203]\n\t"
    "ds_read_b128 v[184:187], v9 offset:34816\n\t"
    "v_cvt_pk_bf16_f32 v138, v140, v141\n\t"
    "v_mfma_f32_16x16x32_bf16 v[156:159], v[88:91], v[40:43], v[156:159]\n\t"
    "ds_read_b64_tr_b16 v[36:37], v1 offset:53248\n\t"
    "ds_read_b128 v[188:191], v10 offset:34816\n\t"
    "v_mfma_f32_16x16x32_bf16 v[156:159], v[92:95], v[44:47], v[156:159]\n\t"
    "s_waitcnt lgkmcnt(11)\n\t"
    "ds_read_b64_tr_b16 v[38:39], v1 offset:53760\n\t"
    "v_xor_b32 v1, 0x10000, v1\n\t"
    "v_mfma_f32_16x16x32_bf16 v[128:131], v[160:163], v[16:19], 0\n\t"
    "ds_read_b64_tr_b16 v[40:41], v2 offset:49152\n\t"
    "v_cvt_pk_bf16_f32 v139, v142, v143\n\t"
    "v_mfma_f32_16x16x32_bf16 v[128:131], v[164:167], v[20:23], v[128:131]\n\t"
    "ds_read_b64_tr_b16 v[42:43], v2 offset:49664\n\t"
    "v_cvt_pk_bf16_f32 v144, v144, v145\n\t"
    "v_mfma_f32_16x16x32_bf16 v[196:199], v[136:139], v[48:51], v[196:199]\n\t"
    "v_cvt_pk_bf16_f32 v145, v146, v147\n\t"
    "v_cvt_pk_bf16_f32 v146, v148, v149\n\t"
    "v_mfma_f32_16x16x32_bf16 v[204:207], v[136:139], v[52:55], v[204:207]\n\t"
    "v_cvt_pk_bf16_f32 v147, v150, v151\n\t"
    "v_cvt_pk_bf16_f32 v152, v152, v153\n\t"
    "v_mfma_f32_16x16x32_bf16 v[132:135], v[168:171], v[16:19], 0\n\t"
    "ds_read_b64_tr_b16 v[44:45], v2 offset:53248\n\t"
    "v_cvt_pk_bf16_f32 v153, v154, v155\n\t"
    "v_mfma_f32_16x16x32_bf16 v[132:135], v[172:175], v[20:23], v[132:135]\n\t"
    "ds_read_b64_tr_b16 v[46:47], v2 offset:53760\n\t"
    "v_xor_b32 v2, 0x10000, v2\n\t"
    "v_mfma_f32_16x16x32_bf16 v[208:211], v[144:147], v[48:51], v[208:211]\n\t"
    "v_cvt_pk_bf16_f32 v154, v156, v157\n\t"
    "v_cvt_pk_bf16_f32 v155, v158, v159\n\t"
    "v_mfma_f32_16x16x32_bf16 v[216:219], v[144:147], v[52:55], v[216:219]\n\t"
    "v_xor_b32 v6, 0x4000, v6\n\t"
    "v_xor_b32 v7, 0x4000, v7\n\t"
    "v_mfma_f32_16x16x32_bf16 v[212:215], v[152:155], v[48:51], v[212:215]\n\t"
    "v_mfma_f32_16x16x32_bf16 v[220:223], v[152:155], v[52:55], v[220:223]\n\t"
    "s_waitcnt lgkmcnt(12)\n\t"
    "v_mfma_f32_16x16x32_bf16 v[136:139], v[160:163], v[24:27], 0\n\t"
    "s_barrier\n\t"
    "ds_read_b64_tr_b16 v[16:17], v3 offset:16384\n\t"
    "v_mfma_f32_16x16x32_bf16 v[136:139], v[164:167], v[28:31], v[136:139]\n\t"
    "ds_read_b64_tr_b16 v[18:19], v3 offset:16896\n\t"
    "ds_read_b128 v[96:99], v6\n\t"
    "v_mfma_f32_16x16x32_bf16 v[140:143], v[168:171], v[24:27], 0\n\t"
    "ds_read_b64_tr_b16 v[20:21], v3 offset:20480\n\t"
    "ds_read_b128 v[100:103], v6 offset:1024\n\t"
    "v_mfma_f32_16x16x32_bf16 v[140:143], v[172:175], v[28:31], v[140:143]\n\t"
    "s_waitcnt lgkmcnt(9)\n\t"
    "ds_read_b64_tr_b16 v[22:23], v3 offset:20992\n\t"
    "ds_read_b32 v8, v7 offset:2048\n\t"
    "v_mfma_f32_16x16x32_bf16 v[128:131], v[176:179], v[32:35], v[128:131]\n\t"
    "ds_read_b64_tr_b16 v[24:25], v4 offset:16384\n\t"
    "v_mfma_f32_16x16x32_bf16 v[128:131], v[180:183], v[36:39], v[128:131]\n\t"
    "ds_read_b64_tr_b16 v[26:27], v4 offset:16896\n\t"
    "s_nop 0\n\t"
    "v_mfma_f32_16x16x32_bf16 v[132:135], v[184:187], v[32:35], v[132:135]\n\t"
    "ds_read_b64_tr_b16 v[28:29], v4 offset:20480\n\t"
    "s_nop 0\n\t"
    "v_mfma_f32_16x16x32_bf16 v[132:135], v[188:191], v[36:39], v[132:135]\n\t"
    "s_waitcnt lgkmcnt(10)\n\t"
    "ds_read_b64_tr_b16 v[30:31], v4 offset:20992\n\t"
    "s_nop 0\n\t"
    "v_mfma_f32_16x16x32_bf16 v[136:139], v[176:179], v[40:43], v[136:139]\n\t"
    "ds_read_b64_tr_b16 v[32:33], v3 offset:49152\n\t"
    "v_cvt_pk_bf16_f32 v128, v128, v129\n\t"
    "v_mfma_f32_16x16x32_bf16 v[136:139], v[180:183], v[44:47], v[136:139]\n\t"
    "ds_read_b64_tr_b16 v[34:35], v3 offset:49664\n\t"
    "v_cvt_pk_bf16_f32 v129, v130, v131\n\t"
    "v_mov_b32 v11, s46\n\t"
    "s_waitcnt lgkmcnt(10)\n\t"
    "v_dot2_u32_u16 v11, v96, s84, v11\n\t"
    "v_dot2_u32_u16 v11, v97, s85, v11\n\t"
    "v_dot2_u32_u16 v11, v98, s86, v11\n\t"
    "v_dot2_u32_u16 v11, v99, s87, v11\n\t"
    "s_waitcnt lgkmcnt(8)\n\t"
    "v_dot2_u32_u16 v11, v100, s88, v11\n\t"
    "v_dot2_u32_u16 v11, v101, s89, v11\n\t"
    "v_dot2_u32_u16 v11, v102, s90, v11\n\t"
    "v_dot2_u32_u16 v11, v103, s91, v11\n\t"
    "s_nop 2\n\t"
    "s_waitcnt lgkmcnt(6)\n\t"
    "v_cmp_ne_u32 vcc, v8, v11\n\t"
    "s_cbranch_vccnz .Lr7_%=\n\t"
    ".Lr7x_%=:\n\t"
    "v_mov_b32 v11, s47\n\t"
    "v_mfma_f32_16x16x32_bf16 v[140:143], v[184:187], v[40:43], v[140:143]\n\t"
    "ds_read_b64_tr_b16 v[36:37], v3 offset:53248\n\t"
    "v_cvt_pk_bf16_f32 v130, v132, v133\n\t"
    "v_mfma_f32_16x16x32_bf16 v[140:143], v[188:191], v[44:47], v[140:143]\n\t"
    "ds_read_b64_tr_b16 v[38:39], v3 offset:53760\n\t"
    "v_xor_b32 v3, 0x10000, v3\n\t"
    "v_mfma_f32_16x16x32_bf16 v[144:147], v[160:163], v[16:19], 0\n\t"
    "ds_read_b64_tr_b16 v[40:41], v4 offset:49152\n\t"
    "v_cvt_pk_bf16_f32 v131, v134, v135\n\t"
    "v_mfma_f32_16x16x32_bf16 v[144:147], v[164:167], v[20:23], v[144:147]\n\t"
    "ds_read_b64_tr_b16 v[42:43], v4 offset:49664\n\t"
    "v_cvt_pk_bf16_f32 v136, v136, v137\n\t"
    "v_mfma_f32_16x16x32_bf16 v[192:195], v[128:131], v[96:99], v[192:195]\n\t"
    "v_cvt_pk_bf16_f32 v137, v138, v139\n\t"
    "v_cvt_pk_bf16_f32 v138, v140, v141\n\t"
    "v_mfma_f32_16x16x32_bf16 v[200:203], v[128:131], v[100:103], v[200:203]\n\t"
    "v_cvt_pk_bf16_f32 v139, v142, v143\n\t"
    "s_nop 0\n\t"
    "v_mfma_f32_16x16x32_bf16 v[148:151], v[168:171], v[16:19], 0\n\t"
    "ds_read_b64_tr_b16 v[44:45], v4 offset:53248\n\t"
    "s_nop 0\n\t"
    "v_mfma_f32_16x16x32_bf16 v[148:151], v[172:175], v[20:23], v[148:151]\n\t"
    ".Lhop12_%=:\n\t"
    "s_cbranch_execz .Lhr_%=\n\t"
    "ds_read_b64_tr_b16 v[46:47], v4 offset:53760\n\t"
    "v_xor_b32 v4, 0x10000, v4\n\t"
    "v_mfma_f32_16x16x32_bf16 v[196:199], v[136:139], v[96:99], v[196:199]\n\t"
    "v_cvt_pk_bf16_f32 v192, v192, v193\n\t"
    "v_cvt_pk_bf16_f32 v193, v194, v195\n\t"
    "v_mfma_f32_16x16x32_bf16 v[204:207], v[136:139], v[100:103], v[204:207]\n\t"
    "s_waitcnt lgkmcnt(8)\n\t"
    "v_cvt_pk_bf16_f32 v200, v200, v201\n\t"
    "v_cvt_pk_bf16_f32 v201, v202, v203\n\t"
    "v_mfma_f32_16x16x32_bf16 v[152:155], v[160:163], v[24:27], 0\n\t"
    "ds_read_b64_tr_b16 v[16:17], v1 offset:24576\n\t"
    "s_nop 0\n\t"
    "v_mfma_f32_16x16x32_bf16 v[152:155], v[164:167], v[28:31], v[152:155]\n\t"
    "ds_read_b64_tr_b16 v[18:19], v1 offset:25088\n\t"
    "v_cvt_pk_bf16_f32 v194, v196, v197\n\t"
    "v_mfma_f32_16x16x32_bf16 v[156:159], v[168:171], v[24:27], 0\n\t"
    "ds_read_b64_tr_b16 v[20:21], v1 offset:28672\n\t"
    "v_cvt_pk_bf16_f32 v195, v198, v199\n\t"
    "v_mfma_f32_16x16x32_bf16 v[156:159], v[172:175], v[28:31], v[156:159]\n\t"
    "s_waitcnt lgkmcnt(7)\n\t"
    "ds_read_b64_tr_b16 v[22:23], v1 offset:29184\n\t"
    "v_cvt_pk_bf16_f32 v202, v204, v205\n\t"
    "buffer_store_dwordx4 v[192:195], v14, s[32:35], s64 offen sc1\n\t"
    "v_mfma_f32_16x16x32_bf16 v[144:147], v[176:179], v[32:35], v[144:147]\n\t"
    "ds_read_b64_tr_b16 v[24:25], v2 offset:24576\n\t"
    "v_cvt_pk_bf16_f32 v203, v206, v207\n\t"
    "v_mfma_f32_16x16x32_bf16 v[144:147], v[180:183], v[36:39], v[144:147]\n\t"
    "ds_read_b64_tr_b16 v[26:27], v2 offset:25088\n\t"
    "buffer_store_dwordx4 v[200:203], v14, s[32:35], s64 offen offset:1024 sc1\n\t"
    "v_dot2_u32_u16 v6, v192, s84, v11\n\t"
    "v_dot2_u32_u16 v6, v193, s85, v6\n\t"
    "v_dot2_u32_u16 v6, v194, s86, v6\n\t"
    "v_dot2_u32_u16 v6, v195, s87, v6\n\t"
    "v_dot2_u32_u16 v6, v200, s88, v6\n\t"
    "v_dot2_u32_u16 v6, v201, s89, v6\n\t"
    "v_dot2_u32_u16 v6, v202, s90, v6\n\t"
    "v_dot2_u32_u16 v6, v203, s91, v6\n\t"
    "v_mfma_f32_16x16x32_bf16 v[148:151], v[184:187], v[32:35], v[148:151]\n\t"
    "ds_read_b64_tr_b16 v[28:29], v2 offset:28672\n\t"
    "v_cvt_pk_bf16_f32 v144, v144, v145\n\t"
    "v_mfma_f32_16x16x32_bf16 v[148:151], v[188:191], v[36:39], v[148:151]\n\t"
    "s_waitcnt lgkmcnt(7)\n\t"
    "ds_read_b64_tr_b16 v[30:31], v2 offset:29184\n\t"
    "v_cvt_pk_bf16_f32 v145, v146, v147\n\t"
    "s_nop 1\n\t"
    "buffer_store_dword v6, v13, s[32:35], s67 offen offset:768 sc1\n\t"
    "v_mfma_f32_16x16x32_bf16 v[152:155], v[176:179], v[40:43], v[152:155]\n\t"
    "ds_read_b64_tr_b16 v[32:33], v1 offset:57344\n\t"
    "s_nop 0\n\t"
    "v_mfma_f32_16x16x32_bf16 v[152:155], v[180:183], v[44:47], v[152:155]\n\t"
    "ds_read_b64_tr_b16 v[34:35], v1 offset:57856\n\t"
    "v_cvt_pk_bf16_f32 v146, v148, v149\n\t"
    "v_mfma_f32_16x16x32_bf16 v[156:159], v[184:187], v[40:43], v[156:159]\n\t"
    "ds_read_b64_tr_b16 v[36:37], v1 offset:61440\n\t"
    "v_cvt_pk_bf16_f32 v147, v150, v151\n\t"
    "v_mfma_f32_16x16x32_bf16 v[156:159], v[188:191], v[44:47], v[156:159]\n\t"
    "ds_read_b64_tr_b16 v[38:39], v1 offset:61952\n\t"
    "v_xor_b32 v1, 0x10000, v1\n\t"
    "v_mfma_f32_16x16x32_bf16 v[208:211], v[144:147], v[96:99], v[208:211]\n\t"
    "v_cvt_pk_bf16_f32 v152, v152, v153\n\t"
    "v_cvt_pk_bf16_f32 v153, v154, v155\n\t"
    "v_mfma_f32_16x16x32_bf16 v[216:219], v[144:147], v[100:103], v[216:219]\n\t"
    "s_waitcnt lgkmcnt(8)\n\t"
    "s_nop 1\n\t"
    "v_mfma_f32_16x16x32_bf16 v[128:131], v[64:67], v[16:19], 0\n\t"
    "ds_read_b64_tr_b16 v[40:41], v2 offset:57344\n\t"
    "v_cvt_pk_bf16_f32 v154, v156, v157\n\t"
    "v_mfma_f32_16x16x32_bf16 v[128:131], v[68:71], v[20:23], v[128:131]\n\t"
    "ds_read_b64_tr_b16 v[42:43], v2 offset:57856\n\t"
    "v_cvt_pk_bf16_f32 v155, v158, v159\n\t"
    "v_mfma_f32_16x16x32_bf16 v[224:227], v[112:115], v[56:59], v[224:227]\n\t"
    "v_cvt_pk_bf16_f32 v208, v208, v209\n\t"
    "v_cvt_pk_bf16_f32 v209, v210, v211\n\t"
    "v_mfma_f32_16x16x32_bf16 v[212:215], v[152:155], v[96:99], v[212:215]\n\t"
    "v_cvt_pk_bf16_f32 v216, v216, v217\n\t"
    "v_cvt_pk_bf16_f32 v217, v218, v219\n\t"
    "v_mfma_f32_16x16x32_bf16 v[220:223], v[152:155], v[100:103], v[220:223]\n\t"
    "s_nop 1\n\t"
    "v_mfma_f32_16x16x32_bf16 v[132:135], v[72:75], v[16:19], 0\n\t"
    "ds_read_b64_tr_b16 v[44:45], v2 offset:61440\n\t"
    "s_nop 0\n\t"
    "v_mfma_f32_16x16x32_bf16 v[132:135], v[76:79], v[20:23], v[132:135]\n\t"
    "ds_read_b64_tr_b16 v[46:47], v2 offset:61952\n\t"
    "v_xor_b32 v2, 0x10000, v2\n\t"
    "v_mfma_f32_16x16x32_bf16 v[232:235], v[112:115], v[60:63], v[232:235]\n\t"
    "s_waitcnt lgkmcnt(8)\n\t"
    "v_cvt_pk_bf16_f32 v210, v212, v213\n\t"
    "v_cvt_pk_bf16_f32 v211, v214, v215\n\t"
    "buffer_store_dwordx4 v[208:211], v14, s[32:35], s63 offen sc1\n\t"
    "v_mfma_f32_16x16x32_bf16 v[136:139], v[64:67], v[24:27], 0\n\t"
    "s_barrier\n\t"
    "ds_read_b64_tr_b16 v[16:17], v1 offset:24576\n\t"
    "v_cvt_pk_bf16_f32 v218, v220, v221\n\t"
    "v_mfma_f32_16x16x32_bf16 v[136:139], v[68:71], v[28:31], v[136:139]\n\t"
    "ds_read_b64_tr_b16 v[18:19], v1 offset:25088\n\t"
    "v_cvt_pk_bf16_f32 v219, v222, v223\n\t"
    "buffer_store_dwordx4 v[216:219], v14, s[32:35], s63 offen offset:1024 sc1\n\t"
    "v_dot2_u32_u16 v7, v208, s84, v11\n\t"
    "v_dot2_u32_u16 v7, v209, s85, v7\n\t"
    "v_dot2_u32_u16 v7, v210, s86, v7\n\t"
    "v_dot2_u32_u16 v7, v211, s87, v7\n\t"
    "v_dot2_u32_u16 v7, v216, s88, v7\n\t"
    "v_dot2_u32_u16 v7, v217, s89, v7\n\t"
    "v_dot2_u32_u16 v7, v218, s90, v7\n\t"
    "v_dot2_u32_u16 v7, v219, s91, v7\n\t"
    "v_mfma_f32_16x16x32_bf16 v[228:231], v[116:119], v[56:59], v[228:231]\n\t"
    "s_nop 1\n\t"
    "buffer_store_dword v7, v13, s[32:35], s66 offen offset:512 sc1\n\t"
    "v_mfma_f32_16x16x32_bf16 v[140:143], v[72:75], v[24:27], 0\n\t"
    "ds_read_b64_tr_b16 v[20:21], v1 offset:28672\n\t"
    "v_mfma_f32_16x16x32_bf16 v[140:143], v[76:79], v[28:31], v[140:143]\n\t"
    "ds_read_b64_tr_b16 v[22:23], v1 offset:29184\n\t"
    "v_mfma_f32_16x16x32_bf16 v[236:239], v[116:119], v[60:63], v[236:239]\n\t"
    "s_waitcnt lgkmcnt(8)\n\t"
    "v_mfma_f32_16x16x32_bf16 v[128:131], v[80:83], v[32:35], v[128:131]\n\t"
    "ds_read_b64_tr_b16 v[24:25], v2 offset:24576\n\t"
    "v_mfma_f32_16x16x32_bf16 v[128:131], v[84:87], v[36:39], v[128:131]\n\t"
    "ds_read_b64_tr_b16 v[26:27], v2 offset:25088\n\t"
    "s_nop 0\n\t"
    "v_mfma_f32_16x16x32_bf16 v[132:135], v[88:91], v[32:35], v[132:135]\n\t"
    "ds_read_b64_tr_b16 v[28:29], v2 offset:28672\n\t"
    "s_nop 0\n\t"
    "v_mfma_f32_16x16x32_bf16 v[132:135], v[92:95], v[36:39], v[132:135]\n\t"
    "s_waitcnt lgkmcnt(7)\n\t"
    "ds_read_b64_tr_b16 v[30:31], v2 offset:29184\n\t"
    "s_nop 0\n\t"
    "v_mfma_f32_16x16x32_bf16 v[136:139], v[80:83], v[40:43], v[136:139]\n\t"
    "ds_read_b64_tr_b16 v[32:33], v1 offset:57344\n\t"
    "v_cvt_pk_bf16_f32 v128, v128, v129\n\t"
    "v_mfma_f32_16x16x32_bf16 v[136:139], v[84:87], v[44:47], v[136:139]\n\t"
    "ds_read_b64_tr_b16 v[34:35], v1 offset:57856\n\t"
    "v_cvt_pk_bf16_f32 v129, v130, v131\n\t"
    "v_mfma_f32_16x16x32_bf16 v[140:143], v[88:91], v[40:43], v[140:143]\n\t"
    ".Lhop13_%=:\n\t"
    "s_cbranch_execz .Lhr_%=\n\t"
    "ds_read_b64_tr_b16 v[36:37], v1 offset:61440\n\t"
    "v_cvt_pk_bf16_f32 v130, v132, v133\n\t"
    "v_mfma_f32_16x16x32_bf16 v[140:143], v[92:95], v[44:47], v[140:143]\n\t"
    "s_waitcnt lgkmcnt(7)\n\t"
    "ds_read_b64_tr_b16 v[38:39], v1 offset:61952\n\t"
    "v_cvt_pk_bf16_f32 v131, v134, v135\n\t"
    "v_mfma_f32_16x16x32_bf16 v[144:147], v[160:163], v[16:19], 0\n\t"
    "ds_read_b64_tr_b16 v[40:41], v2 offset:57344\n\t"
    "v_cvt_pk_bf16_f32 v136, v136, v137\n\t"
    "v_mfma_f32_16x16x32_bf16 v[144:147], v[164:167], v[20:23], v[144:147]\n\t"
    "ds_read_b64_tr_b16 v[42:43], v2 offset:57856\n\t"
    "v_cvt_pk_bf16_f32 v137, v138, v139\n\t"
    "v_mfma_f32_16x16x32_bf16 v[224:227], v[128:131], v[48:51], v[224:227]\n\t"
    "v_cvt_pk_bf16_f32 v138, v140, v141\n\t"
    "v_cvt_pk_bf16_f32 v139, v142, v143\n\t"
    "v_mfma_f32_16x16x32_bf16 v[232:235], v[128:131], v[52:55], v[232:235]\n\t"
    "s_nop 1\n\t"
    "v_mfma_f32_16x16x32_bf16 v[228:231], v[136:139], v[48:51], v[228:231]\n\t"
    "v_mfma_f32_16x16x32_bf16 v[236:239], v[136:139], v[52:55], v[236:239]\n\t"
    "v_mfma_f32_16x16x32_bf16 v[148:151], v[168:171], v[16:19], 0\n\t"
    "ds_read_b64_tr_b16 v[44:45], v2 offset:61440\n\t"
    "v_mfma_f32_16x16x32_bf16 v[148:151], v[172:175], v[20:23], v[148:151]\n\t"
    "s_waitcnt lgkmcnt(7)\n\t"
    "ds_read_b64_tr_b16 v[46:47], v2 offset:61952\n\t"
    "v_mfma_f32_16x16x32_bf16 v[152:155], v[160:163], v[24:27], 0\n\t"
    "ds_read_b64_tr_b16 v[16:17], v3 offset:24576\n\t"
    "v_mfma_f32_16x16x32_bf16 v[152:155], v[164:167], v[28:31], v[152:155]\n\t"
    "ds_read_b64_tr_b16 v[18:19], v3 offset:25088\n\t"
    "v_mfma_f32_16x16x32_bf16 v[156:159], v[168:171], v[24:27], 0\n\t"
    "ds_read_b64_tr_b16 v[20:21], v3 offset:28672\n\t"
    "v_mfma_f32_16x16x32_bf16 v[156:159], v[172:175], v[28:31], v[156:159]\n\t"
    "s_waitcnt lgkmcnt(7)\n\t"
    "ds_read_b64_tr_b16 v[22:23], v3 offset:29184\n\t"
    "v_mfma_f32_16x16x32_bf16 v[144:147], v[176:179], v[32:35], v[144:147]\n\t"
    "ds_read_b64_tr_b16 v[24:25], v4 offset:24576\n\t"
    "v_mfma_f32_16x16x32_bf16 v[144:147], v[180:183], v[36:39], v[144:147]\n\t"
    "ds_read_b64_tr_b16 v[26:27], v4 offset:25088\n\t"
    "s_nop 0\n\t"
    "v_mfma_f32_16x16x32_bf16 v[148:151], v[184:187], v[32:35], v[148:151]\n\t"
    "ds_read_b64_tr_b16 v[28:29], v4 offset:28672\n\t"
    "s_nop 0\n\t"
    "v_mfma_f32_16x16x32_bf16 v[148:151], v[188:191], v[36:39], v[148:151]\n\t"
    "s_waitcnt lgkmcnt(7)\n\t"
    "ds_read_b64_tr_b16 v[30:31], v4 offset:29184\n\t"
    "s_nop 0\n\t"
    "v_mfma_f32_16x16x32_bf16 v[152:155], v[176:179], v[40:43], v[152:155]\n\t"
    "ds_read_b64_tr_b16 v[32:33], v3 offset:57344\n\t"
    "v_cvt_pk_bf16_f32 v144, v144, v145\n\t"
    "v_mfma_f32_16x16x32_bf16 v[152:155], v[180:183], v[44:47], v[152:155]\n\t"
    "ds_read_b64_tr_b16 v[34:35], v3 offset:57856\n\t"
    "v_cvt_pk_bf16_f32 v145, v146, v147\n\t"
    "v_mfma_f32_16x16x32_bf16 v[156:159], v[184:187], v[40:43], v[156:159]\n\t"
    "ds_read_b64_tr_b16 v[36:37], v3 offset:61440\n\t"
    "v_cvt_pk_bf16_f32 v146, v148, v149\n\t"
    "v_mfma_f32_16x16x32_bf16 v[156:159], v[188:191], v[44:47], v[156:159]\n\t"
    "s_waitcnt lgkmcnt(7)\n\t"
    "ds_read_b64_tr_b16 v[38:39], v3 offset:61952\n\t"
    "v_xor_b32 v3, 0x10000, v3\n\t"
    "v_mfma_f32_16x16x32_bf16 v[128:131], v[64:67], v[16:19], 0\n\t"
    "ds_read_b64_tr_b16 v[40:41], v4 offset:57344\n\t"
    "v_cvt_pk_bf16_f32 v147, v150, v151\n\t"
    "v_mfma_f32_16x16x32_bf16 v[128:131], v[68:71], v[20:23], v[128:131]\n\t"
    "ds_read_b64_tr_b16 v[42:43], v4 offset:57856\n\t"
    "v_cvt_pk_bf16_f32 v152, v152, v153\n\t"
    "v_mfma_f32_16x16x32_bf16 v[224:227], v[144:147], v[96:99], v[224:227]\n\t"
    "v_cvt_pk_bf16_f32 v153, v154, v155\n\t"
    "v_cvt_pk_bf16_f32 v154, v156, v157\n\t"
    "v_mfma_f32_16x16x32_bf16 v[232:235], v[144:147], v[100:103], v[232:235]\n\t"
    "v_cvt_pk_bf16_f32 v155, v158, v159\n\t"
    "s_nop 0\n\t"
    "v_mfma_f32_16x16x32_bf16 v[240:243], v[120:123], v[56:59], v[240:243]\n\t"
    "s_nop 1\n\t"
    "v_mfma_f32_16x16x32_bf16 v[228:231], v[152:155], v[96:99], v[228:231]\n\t"
    "v_cvt_pk_bf16_f32 v224, v224, v225\n\t"
    "v_cvt_pk_bf16_f32 v225, v226, v227\n\t"
    "v_mfma_f32_16x16x32_bf16 v[236:239], v[152:155], v[100:103], v[236:239]\n\t"
    "v_cvt_pk_bf16_f32 v232, v232, v233\n\t"
    "v_cvt_pk_bf16_f32 v233, v234, v235\n\t"
    "v_mfma_f32_16x16x32_bf16 v[132:135], v[72:75], v[16:19], 0\n\t"
    "ds_read_b64_tr_b16 v[44:45], v4 offset:61440\n\t"
    "s_nop 0\n\t"
    "v_mfma_f32_16x16x32_bf16 v[132:135], v[76:79], v[20:23], v[132:135]\n\t"
    "ds_read_b64_tr_b16 v[46:47], v4 offset:61952\n\t"
    "v_xor_b32 v4, 0x10000, v4\n\t"
    "v_mfma_f32_16x16x32_bf16 v[248:251], v[120:123], v[60:63], v[248:251]\n\t"
    "s_waitcnt lgkmcnt(8)\n\t"
    "v_cvt_pk_bf16_f32 v226, v228, v229\n\t"
    "v_cvt_pk_bf16_f32 v227, v230, v231\n\t"
    "buffer_store_dwordx4 v[224:227], v14, s[32:35], s62 offen sc1\n\t"
    "v_mfma_f32_16x16x32_bf16 v[136:139], v[64:67], v[24:27], 0\n\t"
    "ds_read_b64_tr_b16 v[16:17], v3 offset:24576\n\t"
    "v_cvt_pk_bf16_f32 v234, v236, v237\n\t"
    "v_mfma_f32_16x16x32_bf16 v[136:139], v[68:71], v[28:31], v[136:139]\n\t"
    "ds_read_b64_tr_b16 v[18:19], v3 offset:25088\n\t"
    "v_cvt_pk_bf16_f32 v235, v238, v239\n\t"
    "buffer_store_dwordx4 v[232:235], v14, s[32:35], s62 offen offset:1024 sc1\n\t"
    "v_dot2_u32_u16 v8, v224, s84, v11\n\t"
    "v_dot2_u32_u16 v8, v225, s85, v8\n\t"
    "v_dot2_u32_u16 v8, v226, s86, v8\n\t"
    "v_dot2_u32_u16 v8, v227, s87, v8\n\t"
    "v_dot2_u32_u16 v8, v232, s88, v8\n\t"
    "v_dot2_u32_u16 v8, v233, s89, v8\n\t"
    "v_dot2_u32_u16 v8, v234, s90, v8\n\t"
    "v_dot2_u32_u16 v8, v235, s91, v8\n\t"
    "v_mfma_f32_16x16x32_bf16 v[244:247], v[124:127], v[56:59], v[244:247]\n\t"
    "s_nop 1\n\t"
    "buffer_store_dword v8, v13, s[32:35], s65 offen offset:256 sc1\n\t"
    "v_mfma_f32_16x16x32_bf16 v[140:143], v[72:75], v[24:27], 0\n\t"
    "ds_read_b64_tr_b16 v[20:21], v3 offset:28672\n\t"
    "v_mfma_f32_16x16x32_bf16 v[140:143], v[76:79], v[28:31], v[140:143]\n\t"
    "ds_read_b64_tr_b16 v[22:23], v3 offset:29184\n\t"
    "v_mfma_f32_16x16x32_bf16 v[252:255], v[124:127], v[60:63], v[252:255]\n\t"
    "s_waitcnt lgkmcnt(8)\n\t"
    "v_mfma_f32_16x16x32_bf16 v[128:131], v[80:83], v[32:35], v[128:131]\n\t"
    "ds_read_b64_tr_b16 v[24:25], v4 offset:24576\n\t"
    "v_mfma_f32_16x16x32_bf16 v[128:131], v[84:87], v[36:39], v[128:131]\n\t"
    "ds_read_b64_tr_b16 v[26:27], v4 offset:25088\n\t"
    "s_nop 0\n\t"
    "v_mfma_f32_16x16x32_bf16 v[132:135], v[88:91], v[32:35], v[132:135]\n\t"
    "ds_read_b64_tr_b16 v[28:29], v4 offset:28672\n\t"
    "s_nop 0\n\t"
    "v_mfma_f32_16x16x32_bf16 v[132:135], v[92:95], v[36:39], v[132:135]\n\t"
    "s_waitcnt lgkmcnt(7)\n\t"
    "ds_read_b64_tr_b16 v[30:31], v4 offset:29184\n\t"
    "s_nop 0\n\t"
    "v_mfma_f32_16x16x32_bf16 v[136:139], v[80:83], v[40:43], v[136:139]\n\t"
    "ds_read_b64_tr_b16 v[32:33], v3 offset:57344\n\t"
    "v_cvt_pk_bf16_f32 v128, v128, v129\n\t"
    "v_mfma_f32_16x16x32_bf16 v[136:139], v[84:87], v[44:47], v[136:139]\n\t"
    "ds_read_b64_tr_b16 v[34:35], v3 offset:57856\n\t"
    "v_cvt_pk_bf16_f32 v129, v130, v131\n\t"
    "v_mfma_f32_16x16x32_bf16 v[140:143], v[88:91], v[40:43], v[140:143]\n\t"
    "ds_read_b64_tr_b16 v[36:37], v3 offset:61440\n\t"
    "v_cvt_pk_bf16_f32 v130, v132, v133\n\t"
    "v_mfma_f32_16x16x32_bf16 v[140:143], v[92:95], v[44:47], v[140:143]\n\t"
    "s_waitcnt lgkmcnt(7)\n\t"
    "ds_read_b64_tr_b16 v[38:39], v3 offset:61952\n\t"
    "v_cvt_pk_bf16_f32 v131, v134, v135\n\t"
    "v_mfma_f32_16x16x32_bf16 v[144:147], v[160:163], v[16:19], 0\n\t"
    "ds_read_b64_tr_b16 v[40:41], v4 offset:57344\n\t"
    "v_cvt_pk_bf16_f32 v136, v136, v137\n\t"
    "v_mfma_f32_16x16x32_bf16 v[144:147], v[164:167], v[20:23], v[144:147]\n\t"
    ".Lhop14_%=:\n\t"
    "s_cbranch_execz .Lhr_%=\n\t"
    "ds_read_b64_tr_b16 v[42:43], v4 offset:57856\n\t"
    "v_cvt_pk_bf16_f32 v137, v138, v139\n\t"
    "v_mfma_f32_16x16x32_bf16 v[240:243], v[128:131], v[48:51], v[240:243]\n\t"
    "v_cvt_pk_bf16_f32 v138, v140, v141\n\t"
    "v_cvt_pk_bf16_f32 v139, v142, v143\n\t"
    "v_mfma_f32_16x16x32_bf16 v[248:251], v[128:131], v[52:55], v[248:251]\n\t"
    "s_nop 1\n\t"
    "v_mfma_f32_16x16x32_bf16 v[244:247], v[136:139], v[48:51], v[244:247]\n\t"
    "v_mfma_f32_16x16x32_bf16 v[252:255], v[136:139], v[52:55], v[252:255]\n\t"
    "v_mfma_f32_16x16x32_bf16 v[148:151], v[168:171], v[16:19], 0\n\t"
    "ds_read_b64_tr_b16 v[44:45], v4 offset:61440\n\t"
    "v_mfma_f32_16x16x32_bf16 v[148:151], v[172:175], v[20:23], v[148:151]\n\t"
    "s_waitcnt lgkmcnt(7)\n\t"
    "ds_read_b64_tr_b16 v[46:47], v4 offset:61952\n\t"
    "v_mfma_f32_16x16x32_bf16 v[152:155], v[160:163], v[24:27], 0\n\t"
    "v_mfma_f32_16x16x32_bf16 v[152:155], v[164:167], v[28:31], v[152:155]\n\t"
    "v_mfma_f32_16x16x32_bf16 v[156:159], v[168:171], v[24:27], 0\n\t"
    "v_mfma_f32_16x16x32_bf16 v[156:159], v[172:175], v[28:31], v[156:159]\n\t"
    "s_waitcnt lgkmcnt(4)\n\t"
    "v_mfma_f32_16x16x32_bf16 v[144:147], v[176:179], v[32:35], v[144:147]\n\t"
    "v_mfma_f32_16x16x32_bf16 v[144:147], v[180:183], v[36:39], v[144:147]\n\t"
    "s_nop 1\n\t"
    "v_mfma_f32_16x16x32_bf16 v[148:151], v[184:187], v[32:35], v[148:151]\n\t"
    "s_nop 1\n\t"
    "v_mfma_f32_16x16x32_bf16 v[148:151], v[188:191], v[36:39], v[148:151]\n\t"
    "s_waitcnt lgkmcnt(0)\n\t"
    "s_nop 1\n\t"
    "v_mfma_f32_16x16x32_bf16 v[152:155], v[176:179], v[40:43], v[152:155]\n\t"
    "v_cvt_pk_bf16_f32 v144, v144, v145\n\t"
    "v_cvt_pk_bf16_f32 v145, v146, v147\n\t"
    "v_mfma_f32_16x16x32_bf16 v[152:155], v[180:183], v[44:47], v[152:155]\n\t"
    "s_nop 1\n\t"
    "v_mfma_f32_16x16x32_bf16 v[156:159], v[184:187], v[40:43], v[156:159]\n\t"
    "v_cvt_pk_bf16_f32 v146, v148, v149\n\t"
    "v_cvt_pk_bf16_f32 v147, v150, v151\n\t"
    "v_mfma_f32_16x16x32_bf16 v[156:159], v[188:191], v[44:47], v[156:159]\n\t"
    "s_nop 1\n\t"
    "v_mfma_f32_16x16x32_bf16 v[240:243], v[144:147], v[96:99], v[240:243]\n\t"
    "v_cvt_pk_bf16_f32 v152, v152, v153\n\t"
    "v_cvt_pk_bf16_f32 v153, v154, v155\n\t"
    "v_mfma_f32_16x16x32_bf16 v[248:251], v[144:147], v[100:103], v[248:251]\n\t"
    "s_nop 1\n\t"
    "v_cvt_pk_bf16_f32 v154, v156, v157\n\t"
    "v_cvt_pk_bf16_f32 v155, v158, v159\n\t"
    "s_nop 1\n\t"
    "v_mfma_f32_16x16x32_bf16 v[244:247], v[152:155], v[96:99], v[244:247]\n\t"
    "v_mfma_f32_16x16x32_bf16 v[252:255], v[152:155], v[100:103], v[252:255]\n\t"
    "s_waitcnt lgkmcnt(0)\n\t"
    "v_cndmask_b32 v66, 0, v12, s[60:61]\n\t"
    "v_mov_b64 v[64:65], 0\n\t"
    "v_mov_b64 v[68:69], 0\n\t"
    "v_sub_u32 v67, v12, v66\n\t"
    "buffer_load_dwordx4 v[196:199], v14, s[32:35], s70 offen sc1\n\t"
    "buffer_load_dwordx4 v[204:207], v14, s[32:35], s70 offen offset:1024 sc1\n\t"
    "buffer_load_dword v107, v13, s[32:35], s51 offen offset:768 sc1\n\t"
    "buffer_load_dwordx4 v[212:215], v14, s[32:35], s69 offen sc1\n\t"
    "buffer_load_dwordx4 v[220:223], v14, s[32:35], s69 offen offset:1024 sc1\n\t"
    "buffer_load_dword v106, v13, s[32:35], s51 offen offset:512 sc1\n\t"
    "buffer_load_dwordx4 v[228:231], v14, s[32:35], s68 offen sc1\n\t"
    "buffer_load_dwordx4 v[236:239], v14, s[32:35], s68 offen offset:1024 sc1\n\t"
    "buffer_load_dword v105, v13, s[32:35], s51 offen offset:256 sc1\n\t"
    "s_waitcnt vmcnt(6)\n\t"
    "v_dot2_u32_u16 v3, v196, s84, v11\n\t"
    "v_dot2_u32_u16 v3, v197, s85, v3\n\t"
    "v_dot2_u32_u16 v3, v198, s86, v3\n\t"
    "v_dot2_u32_u16 v3, v199, s87, v3\n\t"
    "v_dot2_u32_u16 v3, v204, s88, v3\n\t"
    "v_dot2_u32_u16 v3, v205, s89, v3\n\t"
    "v_dot2_u32_u16 v3, v206, s90, v3\n\t"
    "v_dot2_u32_u16 v3, v207, s91, v3\n\t"
    "s_waitcnt vmcnt(3)\n\t"
    "v_dot2_u32_u16 v2, v212, s84, v11\n\t"
    "v_dot2_u32_u16 v2, v213, s85, v2\n\t"
    "v_dot2_u32_u16 v2, v214, s86, v2\n\t"
    "v_dot2_u32_u16 v2, v215, s87, v2\n\t"
    "v_dot2_u32_u16 v2, v220, s88, v2\n\t"
    "v_dot2_u32_u16 v2, v221, s89, v2\n\t"
    "v_dot2_u32_u16 v2, v222, s90, v2\n\t"
    "v_dot2_u32_u16 v2, v223, s91, v2\n\t"
    "s_waitcnt vmcnt(0)\n\t"
    "v_dot2_u32_u16 v1, v228, s84, v11\n\t"
    "v_dot2_u32_u16 v1, v229, s85, v1\n\t"
    "v_dot2_u32_u16 v1, v230, s86, v1\n\t"
    "v_dot2_u32_u16 v1, v231, s87, v1\n\t"
    "v_dot2_u32_u16 v1, v236, s88, v1\n\t"
    "v_dot2_u32_u16 v1, v237, s89, v1\n\t"
    "v_dot2_u32_u16 v1, v238, s90, v1\n\t"
    "v_dot2_u32_u16 v1, v239, s91, v1\n\t"
    "v_cmp_ne_u32 s[74:75], v3, v107\n\t"
    "v_cmp_ne_u32 s[76:77], v2, v106\n\t"
    "s_nop 0\n\t"
    "v_cmp_ne_u32 vcc, v1, v105\n\t"
    "s_or_b64 s[74:75], s[74:75], s[76:77]\n\t"
    "s_or_b64 vcc, vcc, s[74:75]\n\t"
    "s_cbranch_scc1 .Lfr_%=\n\t"
    ".Lfk_%=:\n\t"
    "v_mfma_f32_16x16x32_bf16 v[240:243], v[66:69], v[196:199], v[240:243]\n\t"
    "v_mfma_f32_16x16x32_bf16 v[240:243], v[66:69], v[212:215], v[240:243]\n\t"
    "s_cmp_lg_u32 s25, 0\n\t"
    "s_cbranch_scc1 .Lnb2_%=\n\t"
    "s_mov_b64 exec, 1\n\t"
    "buffer_store_dword v11, v64, s[32:35], s54 offen sc1\n\t"
    "s_mov_b64 exec, -1\n\t"
    ".Lnb2_%=:\n\t"
    "v_mfma_f32_16x16x32_bf16 v[240:243], v[66:69], v[228:231], v[240:243]\n\t"
    "v_mfma_f32_16x16x32_bf16 v[244:247], v[64:67], v[196:199], v[244:247]\n\t"
    "s_cmp_lg_u32 s26, 0\n\t"
    "s_cbranch_scc1 .Lnb1_%=\n\t"
    "s_mov_b64 exec, 1\n\t"
    "v_mov_b32 v4, s46\n\t"
    "buffer_store_dword v4, v64, s[32:35], s53 offen sc1\n\t"
    "s_mov_b64 exec, -1\n\t"
    ".Lnb1_%=:\n\t"
    "v_mfma_f32_16x16x32_bf16 v[244:247], v[64:67], v[212:215], v[244:247]\n\t"
    "v_mfma_f32_16x16x32_bf16 v[244:247], v[64:67], v[228:231], v[244:247]\n\t"
    "v_mfma_f32_16x16x32_bf16 v[248:251], v[66:69], v[204:207], v[248:251]\n\t"
    "v_mfma_f32_16x16x32_bf16 v[248:251], v[66:69], v[220:223], v[248:251]\n\t"
    "v_mfma_f32_16x16x32_bf16 v[248:251], v[66:69], v[236:239], v[248:251]\n\t"
    "v_cvt_pk_bf16_f32 v240, v240, v241\n\t"
    "v_cvt_pk_bf16_f32 v241, v242, v243\n\t"
    "v_mfma_f32_16x16x32_bf16 v[252:255], v[64:67], v[204:207], v[252:255]\n\t"
    "v_mfma_f32_16x16x32_bf16 v[252:255], v[64:67], v[220:223], v[252:255]\n\t"
    "s_nop 0\n\t"
    "v_mfma_f32_16x16x32_bf16 v[252:255], v[64:67], v[236:239], v[252:255]\n\t"
    "v_cvt_pk_bf16_f32 v242, v244, v245\n\t"
    "v_cvt_pk_bf16_f32 v243, v246, v247\n\t"
    "s_nop 0\n\t"
    "v_permlane16_swap_b32 v240, v242\n\t"
    "v_permlane16_swap_b32 v241, v243\n\t"
    "buffer_store_dwordx4 v[240:243], v5, s[40:43], 0 offen\n\t"
    "v_cvt_pk_bf16_f32 v248, v248, v249\n\t"
    "v_cvt_pk_bf16_f32 v249, v250, v251\n\t"
    "v_cvt_pk_bf16_f32 v250, v252, v253\n\t"
    "v_cvt_pk_bf16_f32 v251, v254, v255\n\t"
    "s_nop 0\n\t"
    "v_permlane16_swap_b32 v248, v250\n\t"
    "v_permlane16_swap_b32 v249, v251\n\t"
    "buffer_store_dwordx4 v[248:251], v5, s[40:43], s58 offen\n\t"
    "s_endpgm\n\t"
    ".Lrt_%=:\n\t"
    "s_lshl_b32 s7, s44, 2\n\t"
    "s_lshr_b32 s7, s48, s7\n\t"
    "s_and_b32 s7, s7, 7\n\t"
    "s_lshl_b32 s7, s7, 17\n\t"
    "s_xor_b32 s49, s50, s7\n\t"
    "s_mov_b32 s52, 0\n\t"
    ".Lpq_%=:\n\t"
    "s_sleep 1\n\t"
    "buffer_load_dwordx4 v[56:59], v14, s[32:35], s49 offen sc1\n\t"
    "buffer_load_dwordx4 v[60:63], v14, s[32:35], s49 offen offset:1024 sc1\n\t"
    "buffer_load_dword v8, v13, s[32:35], s49 offen offset:2048 sc1\n\t"
    "v_mov_b32 v11, s46\n\t"
    "s_waitcnt vmcnt(0)\n\t"
    "v_dot2_u32_u16 v11, v56, s84, v11\n\t"
    "v_dot2_u32_u16 v11, v57, s85, v11\n\t"
    "v_dot2_u32_u16 v11, v58, s86, v11\n\t"
    "v_dot2_u32_u16 v11, v59, s87, v11\n\t"
    "v_dot2_u32_u16 v11, v60, s88, v11\n\t"
    ".Lhop15_%=:\n\t"
    "s_cbranch_execz .Lhr_%=\n\t"
    "v_dot2_u32_u16 v11, v61, s89, v11\n\t"
    "v_dot2_u32_u16 v11, v62, s90, v11\n\t"
    "v_dot2_u32_u16 v11, v63, s91, v11\n\t"
    "s_nop 2\n\t"
    "v_cmp_ne_u32 vcc, v8, v11\n\t"
    "s_nop 0\n\t"
    "s_cmp_lg_u64 vcc, 0\n\t"
    "s_cbranch_scc0 .Lpqx_%=\n\t"
    "s_add_u32 s52, s52, 1\n\t"
    "s_cmp_lt_u32 s52, 1048576\n\t"
    "s_cbranch_scc1 .Lpq_%=\n\t"
    ".Lpqx_%=:\n\t"
    "s_cmp_lg_u64 s[82:83], 0\n\t"
    "s_cbranch_scc0 .Lp2_%=\n\t"
    "s_branch .Ldr_%=\n\t"
    ".Lfr_%=:\n\t"
    "s_mov_b32 s52, 0\n\t"
    ".Lfq_%=:\n\t"
    "s_sleep 1\n\t"
    "buffer_load_dwordx4 v[196:199], v14, s[32:35], s70 offen sc1\n\t"
    "buffer_load_dwordx4 v[204:207], v14, s[32:35], s70 offen offset:1024 sc1\n\t"
    "buffer_load_dword v107, v13, s[32:35], s51 offen offset:768 sc1\n\t"
    "buffer_load_dwordx4 v[212:215], v14, s[32:35], s69 offen sc1\n\t"
    "buffer_load_dwordx4 v[220:223], v14, s[32:35], s69 offen offset:1024 sc1\n\t"
    "buffer_load_dword v106, v13, s[32:35], s51 offen offset:512 sc1\n\t"
    "buffer_load_dwordx4 v[228:231], v14, s[32:35], s68 offen sc1\n\t"
    "buffer_load_dwordx4 v[236:239], v14, s[32:35], s68 offen offset:1024 sc1\n\t"
    "buffer_load_dword v105, v13, s[32:35], s51 offen offset:256 sc1\n\t"
    "s_waitcnt vmcnt(6)\n\t"
    "v_dot2_u32_u16 v3, v196, s84, v11\n\t"
    "v_dot2_u32_u16 v3, v197, s85, v3\n\t"
    "v_dot2_u32_u16 v3, v198, s86, v3\n\t"
    "v_dot2_u32_u16 v3, v199, s87, v3\n\t"
    "v_dot2_u32_u16 v3, v204, s88, v3\n\t"
    "v_dot2_u32_u16 v3, v205, s89, v3\n\t"
    "v_dot2_u32_u16 v3, v206, s90, v3\n\t"
    "v_dot2_u32_u16 v3, v207, s91, v3\n\t"
    "s_waitcnt vmcnt(3)\n\t"
    "v_dot2_u32_u16 v2, v212, s84, v11\n\t"
    "v_dot2_u32_u16 v2, v213, s85, v2\n\t"
    "v_dot2_u32_u16 v2, v214, s86, v2\n\t"
    "v_dot2_u32_u16 v2, v215, s87, v2\n\t"
    "v_dot2_u32_u16 v2, v220, s88, v2\n\t"
    "v_dot2_u32_u16 v2, v221, s89, v2\n\t"
    "v_dot2_u32_u16 v2, v222, s90, v2\n\t"
    "v_dot2_u32_u16 v2, v223, s91, v2\n\t"
    "s_waitcnt vmcnt(0)\n\t"
    "v_dot2_u32_u16 v1, v228, s84, v11\n\t"
    "v_dot2_u32_u16 v1, v229, s85, v1\n\t"
    "v_dot2_u32_u16 v1, v230, s86, v1\n\t"
    "v_dot2_u32_u16 v1, v231, s87, v1\n\t"
    "v_dot2_u32_u16 v1, v236, s88, v1\n\t"
    "v_dot2_u32_u16 v1, v237, s89, v1\n\t"
    "v_dot2_u32_u16 v1, v238, s90, v1\n\t"
    "v_dot2_u32_u16 v1, v239, s91, v1\n\t"
    "v_cmp_ne_u32 s[74:75], v3, v107\n\t"
    "v_cmp_ne_u32 s[76:77], v2, v106\n\t"
    "s_nop 0\n\t"
    "v_cmp_ne_u32 vcc, v1, v105\n\t"
    "s_or_b64 s[74:75], s[74:75], s[76:77]\n\t"
    "s_or_b64 vcc, vcc, s[74:75]\n\t"
    "s_cbranch_scc0 .Lfqx_%=\n\t"
    "s_add_u32 s52, s52, 1\n\t"
    "s_cmp_lt_u32 s52, 1048576\n\t"
    "s_cbranch_scc1 .Lfq_%=\n\t"
    ".Lfqx_%=:\n\t"
    "s_branch .Lfk_%=\n\t"
    ".Lcold_%=:\n\t"
    ".Lr6_%=:\n\t"
    "s_bfe_u32 s7, s48, 0x30018\n\t"
    "s_lshl_b32 s7, s7, 17\n\t"
    "s_xor_b32 s49, s50, s7\n\t"
    "s_mov_b32 s52, 0\n\t"
    ".Lr6q_%=:\n\t"
    "s_sleep 1\n\t"
    "buffer_load_dwordx4 v[48:51], v14, s[32:35], s49 offen sc1\n\t"
    "buffer_load_dwordx4 v[52:55], v14, s[32:35], s49 offen offset:1024 sc1\n\t"
    "buffer_load_dword v8, v13, s[32:35], s49 offen offset:2048 sc1\n\t"
    "v_mov_b32 v11, s46\n\t"
    "s_waitcnt vmcnt(0)\n\t"
    "v_dot2_u32_u16 v11, v48, s84, v11\n\t"
    "v_dot2_u32_u16 v11, v49, s85, v11\n\t"
    "v_dot2_u32_u16 v11, v50, s86, v11\n\t"
    "v_dot2_u32_u16 v11, v51, s87, v11\n\t"
    "v_dot2_u32_u16 v11, v52, s88, v11\n\t"
    "v_dot2_u32_u16 v11, v53, s89, v11\n\t"
    "v_dot2_u32_u16 v11, v54, s90, v11\n\t"
    "v_dot2_u32_u16 v11, v55, s91, v11\n\t"
    "s_nop 2\n\t"
    "v_cmp_ne_u32 vcc, v8, v11\n\t"
    "s_nop 0\n\t"
    "s_cmp_lg_u64 vcc, 0\n\t"
    "s_cbranch_scc0 .Lr6qx_%=\n\t"
    "s_add_u32 s52, s52, 1\n\t"
    "s_cmp_lt_u32 s52, 1048576\n\t"
    "s_cbranch_scc1 .Lr6q_%=\n\t"
    ".Lr6qx_%=:\n\t"
    "s_branch .Lr6x_%=\n\t"
    ".Lr7_%=:\n\t"
    "s_bfe_u32 s7, s48, 0x3001c\n\t"
    "s_lshl_b32 s7, s7, 17\n\t"
    "s_xor_b32 s49, s50, s7\n\t"
    "s_mov_b32 s52, 0\n\t"
    ".Lr7q_%=:\n\t"
    "s_sleep 1\n\t"
    "buffer_load_dwordx4 v[96:99], v14, s[32:35], s49 offen sc1\n\t"
    "buffer_load_dwordx4 v[100:103], v14, s[32:35], s49 offen offset:1024 sc1\n\t"
    "buffer_load_dword v8, v13, s[32:35], s49 offen offset:2048 sc1\n\t"
    "v_mov_b32 v11, s46\n\t"
    "s_waitcnt vmcnt(0)\n\t"
    "v_dot2_u32_u16 v11, v96, s84, v11\n\t"
    "v_dot2_u32_u16 v11, v97, s85, v11\n\t"
    "v_dot2_u32_u16 v11, v98, s86, v11\n\t"
    "v_dot2_u32_u16 v11, v99, s87, v11\n\t"
    "v_dot2_u32_u16 v11, v100, s88, v11\n\t"
    "v_dot2_u32_u16 v11, v101, s89, v11\n\t"
    "v_dot2_u32_u16 v11, v102, s90, v11\n\t"
    "v_dot2_u32_u16 v11, v103, s91, v11\n\t"
    "s_nop 2\n\t"
    "v_cmp_ne_u32 vcc, v8, v11\n\t"
    "s_nop 0\n\t"
    "s_cmp_lg_u64 vcc, 0\n\t"
    "s_cbranch_scc0 .Lr7qx_%=\n\t"
    "s_add_u32 s52, s52, 1\n\t"
    "s_cmp_lt_u32 s52, 1048576\n\t"
    "s_cbranch_scc1 .Lr7q_%=\n\t"
    ".Lr7qx_%=:\n\t"
    "s_branch .Lr7x_%=\n\t"
    ".Lhop16_%=:\n\t"
    "s_branch .Lhr_%=\n\t"

    :: "s"(__builtin_amdgcn_kernarg_segment_ptr()), "s"(blockIdx.x), "{v0}"(threadIdx.x), "s"((unsigned)(size_t)(__attribute__((address_space(3))) char*)smem)
    : "v1","v2","v3","v4","v5","v6","v7","v8","v9","v10","v11","v12","v13","v14","v15","v16","v17","v18","v19","v20","v21","v22","v23","v24",
      "v25","v26","v27","v28","v29","v30","v31","v32","v33","v34","v35","v36","v37","v38","v39","v40","v41","v42","v43","v44","v45","v46","v47","v48",
      "v49","v50","v51","v52","v53","v54","v55","v56","v57","v58","v59","v60","v61","v62","v63","v64","v65","v66","v67","v68","v69","v70","v71","v72",
      "v73","v74","v75","v76","v77","v78","v79","v80","v81","v82","v83","v84","v85","v86","v87","v88","v89","v90","v91","v92","v93","v94","v95","v96",
      "v97","v98","v99","v100","v101","v102","v103","v104","v105","v106","v107","v108","v109","v110","v111","v112","v113","v114","v115","v116","v117","v118","v119","v120",
      "v121","v122","v123","v124","v125","v126","v127","v128","v129","v130","v131","v132","v133","v134","v135","v136","v137","v138","v139","v140","v141","v142","v143","v144",
      "v145","v146","v147","v148","v149","v150","v151","v152","v153","v154","v155","v156","v157","v158","v159","v160","v161","v162","v163","v164","v165","v166","v167","v168",
      "v169","v170","v171","v172","v173","v174","v175","v176","v177","v178","v179","v180","v181","v182","v183","v184","v185","v186","v187","v188","v189","v190","v191","v192",
      "v193","v194","v195","v196","v197","v198","v199","v200","v201","v202","v203","v204","v205","v206","v207","v208","v209","v210","v211","v212","v213","v214","v215","v216",
      "v217","v218","v219","v220","v221","v222","v223","v224","v225","v226","v227","v228","v229","v230","v231","v232","v233","v234","v235","v236","v237","v238","v239","v240",
      "v241","v242","v243","v244","v245","v246","v247","v248","v249","v250","v251","v252","v253","v254","v255","s4","s5","s6","s7","s8","s9","s10","s11","s12",
      "s13","s14","s15","s16","s17","s18","s19","s20","s21","s22","s23","s24","s25","s26","s27","s28","s29","s30","s31","s33","s34","s35","s36","s37",
      "s38","s39","s40","s41","s42","s43","s44","s45","s46","s47","s48","s49","s50","s51","s52","s53","s54","s55","s56","s57","s58","s59","s60","s61",
      "s62","s63","s64","s65","s66","s67","s68","s69","s70","s71","s72","s73","s74","s75","s76","s77","s78","s79","s80","s81","s82","s83","s84","s85",
      "s86","s87","s88","s89","s90","s91","s92","s93","s94","s95","s96","s97","s98","s99","vcc","scc","memory");
}

extern "C" void kernel_launch(void* const* d_in, const int* in_sizes, int n_in, void* d_out, int out_size, void* d_ws, size_t ws_size,
                              hipStream_t stream) {
  concat_heads__mha_block_cached__mha_block_cached__near_ridge_kernel<<<256, 512, 0, stream>>>(d_in[0], d_in[1], d_in[2], d_in[3], d_in[4], d_in[5], d_in[6], d_in[7], d_in[8], d_in[9], d_out, d_ws);
}
```
